```python
import torch

def _softmax(x, axis=-1):
    return torch.softmax(x, dim=axis)
def _gelu(x):
    return torch.nn.functional.gelu(x, approximate="tanh")
def _silu(x):
    return torch.nn.functional.silu(x)
def _sigmoid(x):
    return torch.sigmoid(x)
def _layernorm(x, axis=-1, eps=1e-5):
    m = x.mean(dim=axis, keepdim=True)
    v = x.var(dim=axis, keepdim=True, unbiased=False)
    return (x - m) / torch.sqrt(v + eps)
def _scatter_add(tgt, idx, src, axis=0):
    return tgt.clone().index_add_(axis, idx, src)
def _segment_sum(src, seg, n):
    out = torch.zeros((n,) + src.shape[1:], dtype=src.dtype)
    return out.index_add_(0, seg, src)
def _pad(x, pw):
    flat = tuple(v for pair in reversed(pw) for v in pair)
    return torch.nn.functional.pad(x, flat)

kernel_name = 'sequential_preLN__linear_kernel_hedgehog__dense_activation_relu__prompt'

def setup_inputs():

    _w = (lambda a: (lambda t: t.to(torch.bfloat16) if t.is_floating_point() else t)(torch.as_tensor(a)))
    def _A_setup():
        B, S, D, H = 1, 2048, 640, 32
        Dh = D // H
        return {
            "x": _w((lambda *s: torch.randn(*s, dtype=torch.bfloat16) * 0.05)(B, S, D)),
            "wqkv": _w((lambda *s: torch.randn(*s, dtype=torch.bfloat16) * 0.2)(D, 3 * D)),
            "w_phi": _w((lambda *s: torch.randn(*s, dtype=torch.bfloat16) * 0.2)(H, Dh, Dh) * (Dh ** -0.5)),
            "wo": _w((lambda *s: torch.randn(*s, dtype=torch.bfloat16) * 0.05)(D, D)),
        }
    _A_in = _A_setup()

    def _B_setup():
        B, S, D, Dff = 1, 2048, 640, 1728
        return {
            "x": _w((lambda *s: torch.randn(*s, dtype=torch.bfloat16) * 0.05)(B, S, D)),
            "w1": _w((lambda *s: torch.randn(*s, dtype=torch.bfloat16) * 0.05)(D, Dff)),
            "w2": _w((lambda *s: torch.randn(*s, dtype=torch.bfloat16) * 0.05)(Dff, D)),
        }
    _B_in = _B_setup()

    return {
        "A_x": _A_in["x"],
        "A_wqkv": _A_in["wqkv"],
        "A_w_phi": _A_in["w_phi"],
        "A_wo": _A_in["wo"],
        "B_x": _B_in["x"],
        "B_w1": _B_in["w1"],
        "B_w2": _B_in["w2"],
    }

def reference(A_x, A_wqkv, A_w_phi, A_wo, B_x, B_w1, B_w2):
    def _A_ref(x, wqkv, w_phi, wo):
        B, S, D = x.shape
        H = 32; Dh = D // H
        qkv = (lambda x, *d: x.permute(*d))((x @ wqkv).reshape(B, S, 3, H, Dh), 2, 0, 3, 1, 4)
        q, k, v = qkv[0], qkv[1], qkv[2]
        pq = torch.einsum("bhsd,hdf->bhsf", q, w_phi)
        pk = torch.einsum("bhsd,hdf->bhsf", k, w_phi)
        phi_q = torch.concatenate([_softmax(pq, axis=-1), _softmax(-pq, axis=-1)], axis=-1)
        phi_k = torch.concatenate([_softmax(pk, axis=-1), _softmax(-pk, axis=-1)], axis=-1)
        kv = torch.einsum("bhsf,bhsd->bhfd", phi_k, v)
        z  = torch.einsum("bhsf->bhf", phi_k)
        num = torch.einsum("bhqf,bhfd->bhqd", phi_q, kv)
        den = torch.einsum("bhqf,bhf->bhq", phi_q, z)[..., None] + 1e-6
        o = num / den
        return (lambda x, *d: x.permute(*d))(o, 0, 2, 1, 3).reshape(B, S, D) @ wo

    def _B_ref(x, w1, w2):
        h = torch.einsum("bsd,df->bsf", x, w1)
        zero = torch.zeros_like(h)
        h = torch.maximum(h, zero)
        return torch.einsum("bsf,fd->bsd", h, w2)

    x = A_x
    h = x + _A_ref(_layernorm(x, axis=-1), A_wqkv, A_w_phi, A_wo)
    h2 = h + _B_ref(_layernorm(h, axis=-1), B_w1, B_w2)
    return h2
```

```cpp
#include <hip/hip_runtime.h>
#include <hip/hip_bf16.h>
#include <stdint.h>

typedef unsigned short u16;
static __device__ __forceinline__ float bf2f(u16 v){ return __uint_as_float(((unsigned)v)<<16); }
static __device__ __forceinline__ u16 f2bf(float f){ __hip_bfloat16 b = __float2bfloat16(f); return *(u16*)&b; }

constexpr int S=2048, D=640, H=32, DH=20, FF=1728;

__global__ void ln_kernel(const u16* xin, const float* xf, u16* out){
  int row = blockIdx.x; int t = threadIdx.x;
  float v[10]; float s=0;
  for(int i=0;i<10;i++){ int c=t+64*i; v[i]= xin? bf2f(xin[row*D+c]) : xf[row*D+c]; s+=v[i]; }
  for(int o=32;o;o>>=1) s+=__shfl_xor(s,o);
  float m=s/D; float q=0;
  for(int i=0;i<10;i++){ float d=v[i]-m; q+=d*d; }
  for(int o=32;o;o>>=1) q+=__shfl_xor(q,o);
  float r=1.0f/sqrtf(q/D+1e-5f);
  for(int i=0;i<10;i++){ out[row*D+t+64*i]=f2bf((v[i]-m)*r); }
}
__global__ void gemm_kernel(const u16* A,const u16* B,int M,int N,int K,int mode,const u16* res,const float* resf,u16* Cb,float* Cf){
  __shared__ float As[16][17], Bs[16][17];
  int tx=threadIdx.x, ty=threadIdx.y; int row=blockIdx.y*16+ty, col=blockIdx.x*16+tx; float acc=0;
  for(int k0=0;k0<K;k0+=16){
    As[ty][tx]=bf2f(A[row*K+k0+tx]); Bs[ty][tx]=bf2f(B[(k0+ty)*N+col]); __syncthreads();
    for(int k=0;k<16;k++) acc+=As[ty][k]*Bs[k][tx]; __syncthreads();
  }
  if(mode==0) Cb[row*N+col]=f2bf(acc);
  else if(mode==1) Cb[row*N+col]=f2bf(fmaxf(acc,0.f));
  else if(mode==2) Cf[row*N+col]=bf2f(res[row*N+col])+acc;
  else Cb[row*N+col]=f2bf(resf[row*N+col]+acc);
}
__global__ __launch_bounds__(256) void phi_kernel(const u16* qkv,const u16* wphi,float* phi){
  int idx=blockIdx.x*blockDim.x+threadIdx.x; if(idx>=2*S*H) return;
  int h=idx%H; int s=(idx/H)%S; int ty=idx/(H*S);
  float a[20],p[20];
  #pragma unroll
  for(int d=0;d<20;d++) a[d]=bf2f(qkv[s*3*D+ty*D+h*20+d]);
  float mx=-1e30f,mn=1e30f;
  #pragma unroll
  for(int f=0;f<20;f++){ float t=0;
    #pragma unroll
    for(int d=0;d<20;d++) t+=a[d]*bf2f(wphi[h*400+d*20+f]); p[f]=t; mx=fmaxf(mx,t); mn=fminf(mn,t);}
  float s1=0,s2=0;
  #pragma unroll
  for(int f=0;f<20;f++){ s1+=__expf(p[f]-mx); s2+=__expf(mn-p[f]); }
  float* o=phi+((size_t)(ty*S+s)*H+h)*40;
  #pragma unroll
  for(int f=0;f<20;f++){ o[f]=__expf(p[f]-mx)/s1; o[20+f]=__expf(mn-p[f])/s2; }
}
__global__ void kv_kernel(const float* phi,const u16* qkv,float* kv){
  int h=blockIdx.x; int t=threadIdx.x; if(t>=840) return; int f=t/21,d=t%21; float acc=0;
  for(int s=0;s<S;s++){ float pk=phi[((size_t)(S+s)*H+h)*40+f]; float v= d<20? bf2f(qkv[s*3*D+2*D+h*20+d]):1.f; acc+=pk*v; }
  kv[h*840+t]=acc;
}
__global__ void o_kernel(const float* phi,const float* kv,u16* o){
  int idx=blockIdx.x*blockDim.x+threadIdx.x; if(idx>=S*D) return; int s=idx/D,c=idx%D,h=c/20,d=c%20;
  const float* pq=phi+((size_t)s*H+h)*40; float num=0,den=0;
  for(int f=0;f<40;f++){ num+=pq[f]*kv[h*840+f*21+d]; den+=pq[f]*kv[h*840+f*21+20]; }
  o[idx]=f2bf(num/(den+1e-6f));
}

extern "C" void kernel_launch(void* const* d_in, const int* in_sizes, int n_in,
                              void* d_out, int out_size, void* d_ws, size_t ws_size,
                              hipStream_t stream) {
  const u16* x=(const u16*)d_in[0]; const u16* wqkv=(const u16*)d_in[1]; const u16* wphi=(const u16*)d_in[2]; const u16* wo=(const u16*)d_in[3];
  const u16* w1=(const u16*)d_in[5]; const u16* w2=(const u16*)d_in[6];
  char* ws=(char*)d_ws;
  u16* xn=(u16*)ws; ws+=S*D*2;
  u16* qkv=(u16*)ws; ws+=S*3*D*2;
  float* phi=(float*)ws; ws+=(size_t)2*S*H*40*4;
  float* kv=(float*)ws; ws+=H*840*4;
  u16* o=(u16*)ws; ws+=S*D*2;
  float* h=(float*)ws; ws+=S*D*4;
  u16* hn=(u16*)ws; ws+=S*D*2;
  u16* f1=(u16*)ws; ws+=S*FF*2;
  ln_kernel<<<S,64,0,stream>>>(x,nullptr,xn);
  gemm_kernel<<<dim3(3*D/16,S/16),dim3(16,16),0,stream>>>(xn,wqkv,S,3*D,D,0,nullptr,nullptr,qkv,nullptr);
  phi_kernel<<<2*S*H/256,256,0,stream>>>(qkv,wphi,phi);
  kv_kernel<<<H,896,0,stream>>>(phi,qkv,kv);
  o_kernel<<<S*D/256,256,0,stream>>>(phi,kv,o);
  gemm_kernel<<<dim3(D/16,S/16),dim3(16,16),0,stream>>>(o,wo,S,D,D,2,x,nullptr,nullptr,h);
  ln_kernel<<<S,64,0,stream>>>(nullptr,h,hn);
  gemm_kernel<<<dim3(FF/16,S/16),dim3(16,16),0,stream>>>(hn,w1,S,FF,D,1,nullptr,nullptr,f1,nullptr);
  gemm_kernel<<<dim3(D/16,S/16),dim3(16,16),0,stream>>>(f1,w2,S,D,FF,3,nullptr,h,(u16*)d_out,nullptr);
}
```

```cpp
#include <hip/hip_runtime.h>

__global__ __launch_bounds__(256, 1) __attribute__((aligned(4096)))
void fused_block(const void* x, const void* wqkv, const void* wphi, const void* wo,
                 const void* w1, const void* w2, void* out, void* ws) {
  __shared__ __attribute__((aligned(16))) char lds[163840];
  auto ka = __builtin_amdgcn_kernarg_segment_ptr();
  unsigned b = blockIdx.x, tid = threadIdx.x;
  unsigned l0 = (unsigned)(size_t)(__attribute__((address_space(3))) char*)lds;
  asm volatile(
"START_%=:\n"
"s_load_dwordx2 s[12:13], %0, 8\n"
"s_load_dwordx2 s[16:17], %0, 16\n"
"s_load_dwordx2 s[40:41], %0, 56\n"
"s_load_dwordx2 s[8:9], %0, 0\n"
"s_load_dwordx2 s[20:21], %0, 24\n"
"s_load_dwordx2 s[24:25], %0, 32\n"
"s_load_dwordx2 s[28:29], %0, 40\n"
"s_load_dwordx2 s[36:37], %0, 48\n"
"s_getpc_b64 s[58:59]\n"
"s_lshl_b32 s60, %1, 9\n"
"s_and_b32 s58, s58, 0xfffff000\n"
"s_and_b32 s60, s60, 0x7000\n"
"v_lshlrev_b32 v7, 4, %2\n"
"s_add_u32 s58, s58, s60\n"
"s_addc_u32 s59, s59, 0\n"
"s_mov_b32 m0, 162816\n"
"s_nop 0\n"
"global_load_lds_dwordx4 v7, s[58:59]\n"
"s_mov_b32 s48, %1\n"
"v_lshrrev_b32 v7, 6, %2\n"
"v_and_b32 v0, 63, %2\n"
"v_readfirstlane_b32 s49, v7\n"
"s_mov_b32 s14, 2457600\n"
"s_mov_b32 s15, 0x20000\n"
"s_mov_b32 s18, 25600\n"
"s_mov_b32 s19, 0x20000\n"
"s_mov_b32 s42, 26136576\n"
"s_mov_b32 s43, 0x20000\n"
"v_lshlrev_b32 v1, 4, v0\n"
"s_and_b32 s50, s48, 7\n"
"s_lshr_b32 s51, s48, 3\n"
"s_and_b32 s58, s51, 1\n"
"s_lshl_b32 s86, s50, 1\n"
"s_add_u32 s86, s86, s58\n"
"s_lshr_b32 s89, s51, 1\n"
"s_mul_i32 s58, s89, 52\n"
"s_lshr_b32 s58, s58, 8\n"
"s_mul_i32 s59, s58, 5\n"
"s_sub_u32 s59, s89, s59\n"
"s_lshl_b32 s88, s59, 2\n"
"s_add_u32 s88, s88, s49\n"
"s_add_u32 s87, s58, 1\n"
"s_cmp_eq_u32 s58, 2\n"
"s_cselect_b32 s87, 0, s87\n"
"v_mul_u32_u24 v20, 13108, v0\n"
"v_lshrrev_b32 v20, 16, v20\n"
"v_mul_u32_u24 v20, 3760, v20\n"
"v_lshl_add_u32 v16, v0, 4, v20\n"
"v_add_u32 v19, 64, v0\n"
"v_mul_u32_u24 v20, 13108, v19\n"
"v_lshrrev_b32 v20, 16, v20\n"
"v_mul_u32_u24 v20, 3760, v20\n"
"v_lshl_add_u32 v17, v0, 4, v20\n"
"v_add_u32 v19, 128, v0\n"
"v_mul_u32_u24 v20, 13108, v19\n"
"v_lshrrev_b32 v20, 16, v20\n"
"v_mul_u32_u24 v20, 3760, v20\n"
"v_lshl_add_u32 v18, v0, 4, v20\n"
"s_mul_i32 s60, s88, 122880\n"
"s_mul_i32 s61, s87, 1280\n"
"s_add_u32 s60, s60, s61\n"
"s_mul_i32 s61, s86, 80\n"
"s_add_u32 s60, s60, s61\n"
"s_mul_i32 s61, s86, 1600\n"
"s_mul_i32 s97, s49, 4608\n"
"s_mov_b32 m0, s97\n"
"s_waitcnt lgkmcnt(0)\n"
"buffer_load_dwordx4 v16, s[12:15], s60 offen lds\n"
"buffer_load_dwordx4 v17, s[12:15], s60 offen offset:1024 lds\n"
"s_mov_b32 exec_hi, 0\n"
"buffer_load_dwordx4 v18, s[12:15], s60 offen offset:2048 lds\n"
"s_mov_b64 exec, -1\n"
"s_cmp_eq_u32 s87, 2\n"
"s_cbranch_scc1 NA1_%=\n"
"s_add_u32 m0, m0, 2560\n"
"s_nop 0\n"
"buffer_load_dwordx4 v1, s[16:19], s61 offen lds\n"
"buffer_load_dwordx4 v1, s[16:19], s61 offen offset:1024 lds\n"
"NA1_%=:\n"
"buffer_load_dwordx2 v[4:5], off, s[40:43], 0 sc1\n"
"s_mov_b32 s10, 2621440\n"
"s_mov_b32 s11, 0x20000\n"
"s_mov_b32 s22, 819200\n"
"s_mov_b32 s23, 0x20000\n"
"s_mov_b32 s26, 2211840\n"
"s_mov_b32 s27, 0x20000\n"
"s_mov_b32 s30, 2211840\n"
"s_mov_b32 s31, 0x20000\n"
"s_mov_b32 s38, 2621440\n"
"s_mov_b32 s39, 0x20000\n"
"v_add_u32 v2, 0x10000, v1\n"
"v_add_u32 v3, 0x20000, v1\n"
"s_lshr_b32 s58, s50, 1\n"
"s_lshl_b32 s58, s58, 2\n"
"s_lshr_b32 s59, s51, 3\n"
"s_add_u32 s52, s58, s59\n"
"s_mov_b32 s54, s52\n"
"s_and_b32 s58, s50, 1\n"
"s_and_b32 s59, s51, 7\n"
"s_lshl_b32 s60, s58, 3\n"
"s_add_u32 s53, s60, s59\n"
"s_lshl_b32 s58, s58, 2\n"
"s_and_b32 s60, s59, 3\n"
"s_add_u32 s58, s58, s60\n"
"s_lshr_b32 s59, s59, 2\n"
"s_lshl_b32 s59, s59, 3\n"
"s_add_u32 s55, s58, s59\n"
"v_and_b32 v8, 15, v0\n"
"v_lshrrev_b32 v9, 4, v0\n"
"v_lshrrev_b32 v19, 3, v0\n"
"v_mul_u32_u24 v19, 1280, v19\n"
"v_and_b32 v21, 7, v0\n"
"v_xor_b32 v20, v9, v21\n"
"v_lshl_add_u32 v200, v20, 4, v19\n"
"v_xor_b32 v201, 64, v200\n"
"v_add_u32 v201, 9216, v201\n"
"v_add_u32 v202, 18432, v200\n"
"v_add_u32 v203, 18432, v201\n"
"s_lshl_b32 s90, s52, 2\n"
"s_add_u32 s90, s90, s49\n"
"s_mul_i32 s90, s90, 40960\n"
"s_lshl_b32 s91, s49, 12\n"
"s_mov_b32 s3, s90\n"
"s_add_u32 s7, s90, 1280\n"
"s_and_b32 s50, s51, 7\n"
"s_mul_i32 s50, s50, 5\n"
"s_lshr_b32 s50, s50, 2\n"
"s_lshl_b32 s58, s50, 7\n"
"s_add_u32 s90, s90, s58\n"
"s_add_u32 m0, s91, 106496\n"
"s_nop 0\n"
"buffer_load_dwordx4 v200, s[8:11], s90 offen offset:0 lds\n"
"buffer_load_dwordx4 v201, s[8:11], s90 offen offset:1024 lds\n"
"buffer_load_dwordx4 v202, s[8:11], s90 offen offset:2048 lds\n"
"buffer_load_dwordx4 v203, s[8:11], s90 offen offset:3072 lds\n"
"s_add_u32 s90, s90, 128\n"
"s_cmp_eq_u32 s90, s7\n"
"s_cmov_b32 s90, s3\n"
"s_add_u32 m0, s91, 122880\n"
"s_nop 0\n"
"buffer_load_dwordx4 v200, s[8:11], s90 offen offset:0 lds\n"
"buffer_load_dwordx4 v201, s[8:11], s90 offen offset:1024 lds\n"
"buffer_load_dwordx4 v202, s[8:11], s90 offen offset:2048 lds\n"
"buffer_load_dwordx4 v203, s[8:11], s90 offen offset:3072 lds\n"
"s_add_u32 s90, s90, 128\n"
"s_cmp_eq_u32 s90, s7\n"
"s_cmov_b32 s90, s3\n"
"s_mov_b64 exec, 0\n"
"s_cmp_eq_u32 s49, 0\n"
"s_cbranch_scc1 PAD1_%=\n"
"s_cmp_eq_u32 s49, 1\n"
"s_cbranch_scc1 PAD2_%=\n"
"s_cmp_eq_u32 s49, 2\n"
"s_cbranch_scc1 PAD3_%=\n"
"s_branch PAD4_%=\n"
"PADRET_%=:\n"
"s_mov_b64 exec, -1\n"
"s_cmp_lt_u32 s89, 15\n"
"s_cselect_b32 s0, -1, 0\n"
"s_mov_b32 s51, s87\n"
"v_lshrrev_b32 v19, 1, v8\n"
"v_xor_b32 v19, v19, v9\n"
"v_lshlrev_b32 v19, 4, v19\n"
"v_lshl_add_u32 v19, v8, 7, v19\n"
"v_add_u32 v19, 40960, v19\n"
"v_add_u32 v204, s91, v19\n"
"v_xor_b32 v205, 64, v204\n"
"v_add_u32 v206, 0x10000, v204\n"
"v_add_u32 v207, 0x10000, v205\n"
"v_mov_b32 v20, 0\n"
"v_mov_b32 v21, 0\n"
"v_mov_b32 v22, 0\n"
"v_mov_b32 v23, 0\n"
"ds_write_b128 v3, v[20:23] offset:27136\n"
"v_lshrrev_b32 v30, 2, v8\n"
"v_and_b32 v31, 3, v8\n"
"v_lshl_add_u32 v32, v30, 3, v31\n"
"v_mul_u32_u24 v32, 80, v32\n"
"v_add_u32 v32, s97, v32\n"
"v_lshl_add_u32 v24, v9, 4, v32\n"
"v_add_u32 v25, 64, v32\n"
"s_add_u32 s97, s97, 2560\n"
"v_mul_u32_u24 v32, 40, v30\n"
"v_add_u32 v32, s97, v32\n"
"v_and_b32 v33, 1, v0\n"
"v_lshlrev_b32 v33, 4, v33\n"
"v_mul_u32_u24 v34, 320, v9\n"
"v_add_u32 v34, v34, v32\n"
"v_add_u32 v26, v34, v33\n"
"v_add_u32 v27, 32, v34\n"
"v_add_u32 v34, 1280, v32\n"
"v_add_u32 v28, v34, v33\n"
"v_add_u32 v29, 32, v34\n"
"s_mov_b32 s78, 0xff00ff\n"
"s_mov_b32 s79, 0xff0000ff\n"
"s_mov_b32 s80, 0xff00ff\n"
"s_mov_b32 s81, 0xff00ff00\n"
"s_mov_b32 s82, 0xff00\n"
"s_mov_b32 s83, 0x0\n"
"v_mov_b32 v196, 0x3f803f80\n"
"v_mov_b32 v197, 0x3f803f80\n"
"v_mov_b32 v198, 0x3f803f80\n"
"v_mov_b32 v199, 0x3f803f80\n"
"s_mul_i32 s58, s86, 163840\n"
"s_add_u32 s58, s58, 262144\n"
"s_lshl_b32 s59, s88, 9\n"
"s_add_u32 s59, s59, 153600\n"
"s_add_u32 s59, s59, s58\n"
"s_mul_i32 s60, s88, 5120\n"
"s_add_u32 s60, s60, s58\n"
"s_mul_i32 s61, s88, 2560\n"
"s_add_u32 s61, s61, 102400\n"
"s_add_u32 s61, s61, s58\n"
"s_cmp_eq_u32 s87, 0\n"
"s_cselect_b32 s92, s61, s60\n"
"s_cselect_b32 s64, 320, 0\n"
"s_movk_i32 s65, 128\n"
"s_cmov_b32 s65, 448\n"
"s_add_u32 s93, s92, 2048\n"
"s_cmp_eq_u32 s87, 2\n"
"s_cselect_b32 s66, 3072, 0\n"
"s_cselect_b32 s64, 192, s64\n"
"s_cselect_b32 s65, 136, s65\n"
"s_cselect_b32 s67, 32, 0\n"
"s_add_u32 s92, s92, s66\n"
"s_add_u32 s93, s93, s67\n"
"s_add_u32 s94, s59, s64\n"
"s_add_u32 s95, s59, s65\n"
"v_lshlrev_b32 v32, 3, v9\n"
"v_lshl_add_u32 v32, v30, 1, v32\n"
"v_and_b32 v33, 1, v0\n"
"v_add_u32 v32, v32, v33\n"
"v_lshlrev_b32 v32, 4, v32\n"
"s_cmp_eq_u32 s87, 0\n"
"s_cselect_b64 vcc, -1, 0\n"
"v_cndmask_b32 v40, v1, v32, vcc\n"
"v_lshlrev_b32 v7, 4, v9\n"
"s_lshl_b32 s96, s86, 11\n"
"s_lshl_b32 s67, s89, 3\n"
"s_add_u32 s96, s96, s67\n"
"s_add_u32 s96, s96, 4096\n"
"s_waitcnt vmcnt(8)\n"
"v_readfirstlane_b32 s56, v4\n"
"v_readfirstlane_b32 s57, v5\n"
"s_cmp_lg_u32 s51, 2\n"
"s_nop 2\n"
"s_cbranch_scc1 NV2_%=\n"
"v_add_u32 v32, s97, v1\n"
"ds_write_b128 v32, v[20:23]\n"
"ds_write_b128 v32, v[20:23] offset:1024\n"
"v_subrev_u32 v33, 20, v0\n"
"v_cmp_gt_u32 vcc, 20, v0\n"
"s_nop 1\n"
"v_cndmask_b32 v33, v33, v0, vcc\n"
"v_mul_u32_u24 v34, 20, v0\n"
"v_add_u32 v33, v33, v34\n"
"v_lshlrev_b32 v33, 1, v33\n"
"v_add_u32 v33, s97, v33\n"
"s_mov_b32 exec_hi, 0xff\n"
"ds_write_b16 v33, v196\n"
"s_mov_b64 exec, -1\n"
"s_waitcnt lgkmcnt(0)\n"
"NV2_%=:\n"
"ds_read_b64_tr_b16 v[60:61], v26 offset:0\n"
"ds_read_b64_tr_b16 v[62:63], v26 offset:160\n"
"ds_read_b64_tr_b16 v[68:69], v26 offset:8\n"
"ds_read_b64_tr_b16 v[70:71], v26 offset:168\n"
"ds_read_b64_tr_b16 v[76:77], v27 offset:0\n"
"ds_read_b64_tr_b16 v[78:79], v27 offset:160\n"
"ds_read_b64_tr_b16 v[64:65], v28 offset:0\n"
"ds_read_b64_tr_b16 v[66:67], v28 offset:160\n"
"ds_read_b64_tr_b16 v[72:73], v28 offset:8\n"
"ds_read_b64_tr_b16 v[74:75], v28 offset:168\n"
"ds_read_b64_tr_b16 v[80:81], v29 offset:0\n"
"ds_read_b64_tr_b16 v[82:83], v29 offset:160\n"
"ds_read_b128 v[44:47], v24 offset:0\n"
"ds_read_b128 v[52:55], v24 offset:320\n"
"ds_read_b128 v[48:51], v25 offset:0\n"
"ds_read_b128 v[56:59], v25 offset:320\n"
"s_add_u32 s56, s56, 0x7f4a7c15\n"
"s_addc_u32 s57, s57, 0x9e3779b9\n"
"v_mov_b32 v4, s56\n"
"v_mov_b32 v5, s57\n"
"s_waitcnt lgkmcnt(15)\n"
"v_cndmask_b32 v60, 0, v60, s[78:79]\n"
"v_cndmask_b32 v61, 0, v61, s[78:79]\n"
"s_waitcnt lgkmcnt(14)\n"
"v_cndmask_b32 v62, 0, v62, s[80:81]\n"
"v_cndmask_b32 v63, 0, v63, s[80:81]\n"
"s_waitcnt lgkmcnt(13)\n"
"v_cndmask_b32 v68, 0, v68, s[78:79]\n"
"v_cndmask_b32 v69, 0, v69, s[78:79]\n"
"s_waitcnt lgkmcnt(12)\n"
"v_cndmask_b32 v70, 0, v70, s[80:81]\n"
"v_cndmask_b32 v71, 0, v71, s[80:81]\n"
"s_waitcnt lgkmcnt(3)\n"
"v_mfma_f32_16x16x32_bf16 v[84:87], v[44:47], v[60:63], 0\n"
"s_waitcnt lgkmcnt(2)\n"
"v_mfma_f32_16x16x32_bf16 v[88:91], v[52:55], v[60:63], 0\n"
"v_mov_b32_dpp v76, v76 quad_perm:[2,3,2,3] row_mask:0xf bank_mask:0xa\n"
"v_cndmask_b32 v76, 0, v76, s[78:79]\n"
"v_mov_b32_dpp v77, v77 quad_perm:[2,3,2,3] row_mask:0xf bank_mask:0xa\n"
"v_cndmask_b32 v77, 0, v77, s[78:79]\n"
"v_mov_b32_dpp v78, v78 quad_perm:[2,3,2,3] row_mask:0xf bank_mask:0xa\n"
"v_cndmask_b32 v78, 0, v78, s[80:81]\n"
"v_mov_b32_dpp v79, v79 quad_perm:[2,3,2,3] row_mask:0xf bank_mask:0xa\n"
"v_cndmask_b32 v79, 0, v79, s[80:81]\n"
"v_mfma_f32_16x16x32_bf16 v[92:95], v[44:47], v[68:71], 0\n"
"v_mfma_f32_16x16x32_bf16 v[96:99], v[52:55], v[68:71], 0\n"
"v_cndmask_b32 v64, 0, v64, s[82:83]\n"
"v_cndmask_b32 v65, 0, v65, s[82:83]\n"
"v_cndmask_b32 v66, 0, v66, s[82:83]\n"
"v_cndmask_b32 v67, 0, v67, s[82:83]\n"
"v_mfma_f32_16x16x32_bf16 v[100:103], v[44:47], v[76:79], 0\n"
"v_mfma_f32_16x16x32_bf16 v[104:107], v[52:55], v[76:79], 0\n"
"v_cndmask_b32 v72, 0, v72, s[82:83]\n"
"v_cndmask_b32 v73, 0, v73, s[82:83]\n"
"v_cndmask_b32 v74, 0, v74, s[82:83]\n"
"v_cndmask_b32 v75, 0, v75, s[82:83]\n"
"s_waitcnt lgkmcnt(1)\n"
"v_mfma_f32_16x16x32_bf16 v[84:87], v[48:51], v[64:67], v[84:87]\n"
"s_waitcnt lgkmcnt(0)\n"
"v_mfma_f32_16x16x32_bf16 v[88:91], v[56:59], v[64:67], v[88:91]\n"
"v_mov_b32_dpp v80, v80 quad_perm:[2,3,2,3] row_mask:0xf bank_mask:0xa\n"
"v_cndmask_b32 v80, 0, v80, s[82:83]\n"
"v_mov_b32_dpp v81, v81 quad_perm:[2,3,2,3] row_mask:0xf bank_mask:0xa\n"
"v_cndmask_b32 v81, 0, v81, s[82:83]\n"
"v_mov_b32_dpp v82, v82 quad_perm:[2,3,2,3] row_mask:0xf bank_mask:0xa\n"
"v_cndmask_b32 v82, 0, v82, s[82:83]\n"
"v_mov_b32_dpp v83, v83 quad_perm:[2,3,2,3] row_mask:0xf bank_mask:0xa\n"
"v_cndmask_b32 v83, 0, v83, s[82:83]\n"
"v_mfma_f32_16x16x32_bf16 v[92:95], v[48:51], v[72:75], v[92:95]\n"
"v_mfma_f32_16x16x32_bf16 v[96:99], v[56:59], v[72:75], v[96:99]\n"
"v_mfma_f32_16x16x32_bf16 v[100:103], v[48:51], v[80:83], v[100:103]\n"
"v_mfma_f32_16x16x32_bf16 v[104:107], v[56:59], v[80:83], v[104:107]\n"
"s_mov_b32 exec_lo, s0\n"
"s_mov_b32 exec_hi, s0\n"
"v_cvt_pk_bf16_f32 v44, v84, v85\n"
"v_cvt_pk_bf16_f32 v45, v86, v87\n"
"v_cvt_pk_bf16_f32 v46, v88, v89\n"
"v_cvt_pk_bf16_f32 v47, v90, v91\n"
"buffer_store_dwordx4 v[44:47], v1, s[40:43], s92 offen offset:0 sc1\n"
"v_cvt_pk_bf16_f32 v48, v92, v93\n"
"v_cvt_pk_bf16_f32 v49, v94, v95\n"
"v_cvt_pk_bf16_f32 v50, v96, v97\n"
"v_cvt_pk_bf16_f32 v51, v98, v99\n"
"buffer_store_dwordx4 v[48:51], v1, s[40:43], s92 offen offset:1024 sc1\n"
"v_cvt_pk_bf16_f32 v52, v100, v101\n"
"v_cvt_pk_bf16_f32 v53, v102, v103\n"
"v_cvt_pk_bf16_f32 v54, v104, v105\n"
"v_cvt_pk_bf16_f32 v55, v106, v107\n"
"v_mfma_f32_16x16x32_bf16 v[60:63], v[44:47], v[196:199], 0\n"
"v_mfma_f32_16x16x32_bf16 v[64:67], v[48:51], v[196:199], 0\n"
"v_mfma_f32_16x16x32_bf16 v[68:71], v[52:55], v[196:199], 0\n"
"s_and_b32 exec_lo, s0, 0x33333333\n"
"s_mov_b32 exec_hi, exec_lo\n"
"buffer_store_dwordx4 v[52:55], v40, s[40:43], s93 offen sc1\n"
"s_and_b32 exec_lo, s0, 0x10001\n"
"s_mov_b32 exec_hi, exec_lo\n"
"s_nop 0\n"
"buffer_store_dwordx4 v[60:63], v7, s[40:43], s94 offen sc1\n"
"buffer_store_dwordx4 v[64:67], v7, s[40:43], s94 offen offset:64 sc1\n"
"buffer_store_dwordx2 v[68:69], v7, s[40:43], s95 offen sc1\n"
"s_mov_b64 exec, -1\n"
"s_mov_b32 s47, 0x20000\n"
"s_cmp_ge_u32 s48, 108\n"
"s_cbranch_scc1 U3_%=\n"
"s_mov_b64 s[44:45], s[24:25]\n"
"s_mov_b32 s46, s26\n"
"s_mul_i32 s66, s48, 19\n"
"s_lshr_b32 s66, s66, 9\n"
"s_mul_i32 s67, s66, 27\n"
"s_sub_u32 s67, s48, s67\n"
"s_mul_i32 s58, s66, 40\n"
"s_add_u32 s58, s58, s49\n"
"s_mul_i32 s58, s58, 13824\n"
"s_lshl_b32 s59, s67, 7\n"
"s_add_u32 s58, s58, s59\n"
"s_add_u32 s68, s58, 0\n"
"s_add_u32 s69, s58, 110592\n"
"s_add_u32 s70, s58, 276480\n"
"s_add_u32 s71, s58, 387072\n"
"s_add_u32 s72, s58, 221184\n"
"s_lshl_b32 s67, s67, 2\n"
"s_add_u32 s67, s67, s49\n"
"s_cmp_ge_u32 s67, 54\n"
"s_cselect_b32 s62, 54, 0\n"
"s_cselect_b32 s63, 8, 0\n"
"s_sub_u32 s67, s67, s62\n"
"s_mul_i32 s59, s67, 37\n"
"s_lshr_b32 s59, s59, 8\n"
"s_cmp_ge_u32 s67, 48\n"
"s_cselect_b32 s59, 7, s59\n"
"s_mul_i32 s60, s59, 7\n"
"s_cselect_b32 s60, 48, s60\n"
"s_sub_u32 s61, s67, s60\n"
"s_sub_u32 s62, s61, 1\n"
"s_cselect_b32 s62, 6, s62\n"
"s_and_b32 s60, s60, 1\n"
"s_cselect_b32 s61, s62, s61\n"
"s_add_u32 s59, s59, s63\n"
"s_mul_i32 s59, s59, 153600\n"
"s_add_u32 s59, s59, 3702784\n"
"s_mul_i32 s62, s66, 35\n"
"s_add_u32 s62, s62, s61\n"
"s_lshl_b32 s62, s62, 10\n"
"s_add_u32 s63, s59, s62\n"
"s_mul_i32 s62, s66, 40\n"
"s_add_u32 s62, s62, s61\n"
"s_lshl_b32 s62, s62, 6\n"
"s_add_u32 s62, s62, 143360\n"
"s_add_u32 s4, s59, s62\n"
"s_mov_b32 s5, 0x10001\n"
"s_movk_i32 s64, 7168\n"
"s_mov_b32 s65, 0\n"
"s_mov_b32 s59, 55296\n"
"s_mov_b32 s60, 276480\n"
"s_movk_i32 s61, 6912\n"
"s_movk_i32 s62, 3456\n"
"s_lshr_b32 s66, s49, 1\n"
"s_lshl_b32 s66, s66, 6\n"
"s_and_b32 s2, s49, 1\n"
"s_lshl_b32 s2, s2, 3\n"
"s_add_u32 s2, s2, s66\n"
"s_mov_b32 s1, 16\n"
"s_branch U5_%=\n"
"U3_%=:\n"
"s_and_b32 s67, s48, 1\n"
"s_movk_i32 s59, 5120\n"
"s_movk_i32 s61, 2560\n"
"s_cmp_ge_u32 s48, 216\n"
"s_cbranch_scc1 U4_%=\n"
"s_mov_b64 s[44:45], s[28:29]\n"
"s_mov_b32 s46, s30\n"
"s_sub_u32 s66, s48, 108\n"
"s_lshr_b32 s66, s66, 1\n"
"s_lshl_b32 s58, s66, 2\n"
"s_add_u32 s58, s58, s49\n"
"s_lshl_b32 s58, s58, 3\n"
"s_mul_i32 s63, s67, 216\n"
"s_add_u32 s63, s63, s66\n"
"s_mul_i32 s63, s63, 5120\n"
"s_add_u32 s63, s63, 6160384\n"
"s_mov_b32 s65, 271360\n"
"s_branch U6_%=\n"
"U4_%=:\n"
"s_mov_b64 s[44:45], s[20:21]\n"
"s_mov_b32 s46, s22\n"
"s_sub_u32 s66, s48, 216\n"
"s_lshr_b32 s66, s66, 1\n"
"s_and_b32 s62, s49, 1\n"
"s_mul_i32 s58, s66, 40\n"
"s_lshl_b32 s60, s62, 3\n"
"s_add_u32 s58, s58, s60\n"
"s_mul_i32 s60, s66, 160\n"
"s_lshl_b32 s62, s62, 1\n"
"s_add_u32 s60, s60, s62\n"
"s_sub_u32 s60, s60, 2544\n"
"s_cmp_ge_u32 s66, 16\n"
"s_cselect_b32 s58, s60, s58\n"
"s_cmov_b32 s59, 102400\n"
"s_cmov_b32 s61, 51200\n"
"s_lshr_b32 s62, s49, 1\n"
"s_mul_i32 s62, s62, 20\n"
"s_add_u32 s58, s58, s62\n"
"s_mul_i32 s63, s67, 80\n"
"s_add_u32 s63, s63, s66\n"
"s_mul_i32 s63, s63, 5120\n"
"s_add_u32 s63, s63, 2883584\n"
"s_mov_b32 s65, 97280\n"
"U6_%=:\n"
"s_mul_i32 s58, s58, 1280\n"
"s_mul_i32 s67, s67, 640\n"
"s_add_u32 s58, s58, s67\n"
"s_add_u32 s68, s58, 0\n"
"s_add_u32 s69, s58, 128\n"
"s_add_u32 s70, s58, 256\n"
"s_add_u32 s71, s58, 384\n"
"s_add_u32 s72, s58, 512\n"
"s_lshl_b32 s66, s49, 10\n"
"s_add_u32 s63, s63, s66\n"
"s_movk_i32 s64, 4096\n"
"s_mov_b32 s60, s59\n"
"s_movk_i32 s62, 1280\n"
"s_mov_b32 s5, 0\n"
"s_mov_b32 s4, 21811200\n"
"s_lshl_b32 s2, s49, 5\n"
"s_mov_b32 s1, 8\n"
"U5_%=:\n"
"s_add_u32 m0, s91, 139264\n"
"s_nop 0\n"
"buffer_load_dwordx4 v200, s[8:11], s90 offen offset:0 lds\n"
"v_bfe_u32 v208, v0, 4, 1\n"
"v_bfe_u32 v209, v0, 3, 1\n"
"v_and_b32 v210, 7, v0\n"
"v_lshlrev_b32 v210, 4, v210\n"
"v_lshrrev_b32 v211, 5, v0\n"
"v_mad_u32_u24 v210, v208, s61, v210\n"
"buffer_load_dwordx4 v201, s[8:11], s90 offen offset:1024 lds\n"
"v_mad_u32_u24 v210, v209, s62, v210\n"
"v_mad_u32_u24 v186, v211, s59, v210\n"
"v_mad_u32_u24 v187, v211, s60, v210\n"
"s_add_u32 s66, s49, 0\n"
"s_mul_i32 s66, s66, 52\n"
"buffer_load_dwordx4 v202, s[8:11], s90 offen offset:2048 lds\n"
"s_lshr_b32 s66, s66, 8\n"
"s_mul_i32 s66, s66, s65\n"
"s_add_u32 s73, s63, s66\n"
"s_add_u32 s63, s63, s64\n"
"s_add_u32 s66, s49, 4\n"
"s_mul_i32 s66, s66, 52\n"
"buffer_load_dwordx4 v203, s[8:11], s90 offen offset:3072 lds\n"
"s_add_u32 s90, s90, 128\n"
"s_cmp_eq_u32 s90, s7\n"
"s_cmov_b32 s90, s3\n"
"s_lshr_b32 s66, s66, 8\n"
"s_mul_i32 s66, s66, s65\n"
"s_add_u32 s74, s63, s66\n"
"s_add_u32 s63, s63, s64\n"
"s_add_u32 s66, s49, 8\n"
"s_add_u32 m0, s91, 40960\n"
"s_nop 0\n"
"buffer_load_dwordx4 v200, s[8:11], s90 offen offset:0 lds\n"
"s_mul_i32 s66, s66, 52\n"
"s_lshr_b32 s66, s66, 8\n"
"s_mul_i32 s66, s66, s65\n"
"s_add_u32 s75, s63, s66\n"
"s_add_u32 s63, s63, s64\n"
"s_add_u32 s66, s49, 12\n"
"buffer_load_dwordx4 v201, s[8:11], s90 offen offset:1024 lds\n"
"s_mul_i32 s66, s66, 52\n"
"s_lshr_b32 s66, s66, 8\n"
"s_mul_i32 s66, s66, s65\n"
"s_add_u32 s76, s63, s66\n"
"s_add_u32 s63, s63, s64\n"
"buffer_load_dwordx4 v202, s[8:11], s90 offen offset:2048 lds\n"
"s_add_u32 s66, s49, 16\n"
"s_mul_i32 s66, s66, 52\n"
"s_lshr_b32 s66, s66, 8\n"
"s_mul_i32 s66, s66, s65\n"
"s_add_u32 s77, s63, s66\n"
"v_lshl_add_u32 v210, v9, 3, v30\n"
"buffer_load_dwordx4 v203, s[8:11], s90 offen offset:3072 lds\n"
"s_add_u32 s90, s90, 128\n"
"s_cmp_eq_u32 s90, s7\n"
"s_cmov_b32 s90, s3\n"
"v_lshlrev_b32 v210, 7, v210\n"
"v_mad_u32_u24 v210, v31, s1, v210\n"
"s_add_u32 s2, s2, 20480\n"
"v_add_u32 v219, s2, v210\n"
"s_lshl_b32 s1, s49, 10\n"
"s_waitcnt vmcnt(8)\n"
"s_barrier\n"
"s_cmp_lg_u32 s49, 0\n"
"s_cbranch_scc1 FS7_%=\n"
"s_mov_b64 exec, 1\n"
"buffer_store_dwordx2 v[4:5], off, s[40:43], s96 sc1\n"
"s_mov_b64 exec, -1\n"
"FS7_%=:\n"
"ds_read_b128 a[0:3], v206 offset:0\n"
"ds_read_b128 a[4:7], v206 offset:2048\n"
"ds_read_b128 a[8:11], v207 offset:0\n"
"ds_read_b128 a[12:15], v207 offset:2048\n"
"ds_read_b128 a[16:19], v206 offset:16384\n"
"ds_read_b128 a[20:23], v206 offset:18432\n"
"ds_read_b128 a[24:27], v207 offset:16384\n"
"ds_read_b128 a[28:31], v207 offset:18432\n"
"s_waitcnt lgkmcnt(7)\n"
"v_mfma_f32_16x16x32_bf16 v[188:191], a[0:3], a[0:3], 0\n"
"s_add_u32 m0, s91, 57344\n"
"s_nop 0\n"
"buffer_load_dwordx4 v200, s[8:11], s90 offen offset:0 lds\n"
"v_mfma_f32_16x16x32_bf16 v[248:251], v[196:199], a[0:3], 0\n"
"s_waitcnt lgkmcnt(6)\n"
"v_mfma_f32_16x16x32_bf16 v[192:195], a[4:7], a[4:7], 0\n"
"buffer_load_dwordx4 v201, s[8:11], s90 offen offset:1024 lds\n"
"v_mfma_f32_16x16x32_bf16 v[252:255], v[196:199], a[4:7], 0\n"
"s_waitcnt lgkmcnt(5)\n"
"v_mfma_f32_16x16x32_bf16 v[188:191], a[8:11], a[8:11], v[188:191]\n"
"buffer_load_dwordx4 v202, s[8:11], s90 offen offset:2048 lds\n"
"v_mfma_f32_16x16x32_bf16 v[248:251], v[196:199], a[8:11], v[248:251]\n"
"s_waitcnt lgkmcnt(4)\n"
"v_mfma_f32_16x16x32_bf16 v[192:195], a[12:15], a[12:15], v[192:195]\n"
"buffer_load_dwordx4 v203, s[8:11], s90 offen offset:3072 lds\n"
"s_add_u32 s90, s90, 128\n"
"s_cmp_eq_u32 s90, s7\n"
"s_cmov_b32 s90, s3\n"
"v_mfma_f32_16x16x32_bf16 v[252:255], v[196:199], a[12:15], v[252:255]\n"
"s_waitcnt lgkmcnt(3)\n"
"v_mfma_f32_16x16x32_bf16 v[188:191], a[16:19], a[16:19], v[188:191]\n"
"v_mfma_f32_16x16x32_bf16 v[248:251], v[196:199], a[16:19], v[248:251]\n"
"s_waitcnt lgkmcnt(2)\n"
"v_mfma_f32_16x16x32_bf16 v[192:195], a[20:23], a[20:23], v[192:195]\n"
"v_mfma_f32_16x16x32_bf16 v[252:255], v[196:199], a[20:23], v[252:255]\n"
"s_waitcnt lgkmcnt(1)\n"
"v_mfma_f32_16x16x32_bf16 v[188:191], a[24:27], a[24:27], v[188:191]\n"
"v_mfma_f32_16x16x32_bf16 v[248:251], v[196:199], a[24:27], v[248:251]\n"
"s_waitcnt lgkmcnt(0)\n"
"v_mfma_f32_16x16x32_bf16 v[192:195], a[28:31], a[28:31], v[192:195]\n"
"v_mfma_f32_16x16x32_bf16 v[252:255], v[196:199], a[28:31], v[252:255]\n"
"s_add_u32 m0, s91, 73728\n"
"s_nop 0\n"
"buffer_load_dwordx4 v200, s[8:11], s90 offen offset:0 lds\n"
"buffer_load_dwordx4 v201, s[8:11], s90 offen offset:1024 lds\n"
"buffer_load_dwordx4 v202, s[8:11], s90 offen offset:2048 lds\n"
"buffer_load_dwordx4 v203, s[8:11], s90 offen offset:3072 lds\n"
"s_add_u32 s90, s90, 128\n"
"s_cmp_eq_u32 s90, s7\n"
"s_cmov_b32 s90, s3\n"
"s_add_u32 m0, s91, 90112\n"
"s_nop 0\n"
"buffer_load_dwordx4 v200, s[8:11], s90 offen offset:0 lds\n"
"buffer_load_dwordx4 v201, s[8:11], s90 offen offset:1024 lds\n"
"buffer_load_dwordx4 v202, s[8:11], s90 offen offset:2048 lds\n"
"buffer_load_dwordx4 v203, s[8:11], s90 offen offset:3072 lds\n"
"s_add_u32 s90, s90, 128\n"
"s_cmp_eq_u32 s90, s7\n"
"s_cmov_b32 s90, s3\n"
"s_add_u32 m0, s1, 20480\n"
"s_nop 0\n"
"buffer_load_dwordx4 v186, s[44:47], s68 offen lds\n"
"s_add_u32 m0, s1, 24576\n"
"s_nop 0\n"
"buffer_load_dwordx4 v186, s[44:47], s69 offen lds\n"
"s_add_u32 m0, s1, 28672\n"
"s_nop 0\n"
"buffer_load_dwordx4 v186, s[44:47], s70 offen lds\n"
"s_add_u32 m0, s1, 32768\n"
"s_nop 0\n"
"buffer_load_dwordx4 v186, s[44:47], s71 offen lds\n"
"s_add_u32 m0, s1, 36864\n"
"s_nop 0\n"
"buffer_load_dwordx4 v187, s[44:47], s72 offen lds\n"
"s_waitcnt vmcnt(21)\n"
"ds_read_b128 a[32:35], v206 offset:32768\n"
"ds_read_b128 a[36:39], v206 offset:34816\n"
"ds_read_b128 a[40:43], v207 offset:32768\n"
"ds_read_b128 a[44:47], v207 offset:34816\n"
"s_waitcnt lgkmcnt(0)\n"
"s_add_u32 m0, s91, 106496\n"
"s_nop 0\n"
"buffer_load_dwordx4 v200, s[8:11], s90 offen offset:0 lds\n"
"s_mul_i32 s12, s53, 163840\n"
"buffer_load_dwordx4 v201, s[8:11], s90 offen offset:1024 lds\n"
"s_add_u32 s12, s12, 262144\n"
"buffer_load_dwordx4 v202, s[8:11], s90 offen offset:2048 lds\n"
"s_mul_i32 s13, s49, 5120\n"
"buffer_load_dwordx4 v203, s[8:11], s90 offen offset:3072 lds\n"
"s_add_u32 s90, s90, 128\n"
"s_cmp_eq_u32 s90, s7\n"
"s_cmov_b32 s90, s3\n"
"s_mul_i32 s14, s50, 10240\n"
"v_mfma_f32_16x16x32_bf16 v[188:191], a[32:35], a[32:35], v[188:191]\n"
"s_add_u32 s14, s14, s13\n"
"v_mfma_f32_16x16x32_bf16 v[248:251], v[196:199], a[32:35], v[248:251]\n"
"v_lshlrev_b32 v10, 3, v8\n"
"v_mfma_f32_16x16x32_bf16 v[192:195], a[36:39], a[36:39], v[192:195]\n"
"s_lshl_b32 s15, s53, 11\n"
"v_mfma_f32_16x16x32_bf16 v[252:255], v[196:199], a[36:39], v[252:255]\n"
"s_add_u32 s15, s15, 4096\n"
"v_mfma_f32_16x16x32_bf16 v[188:191], a[40:43], a[40:43], v[188:191]\n"
"s_add_u32 s26, s12, s14\n"
"v_mfma_f32_16x16x32_bf16 v[248:251], v[196:199], a[40:43], v[248:251]\n"
"s_add_u32 s58, s14, 20480\n"
"v_mfma_f32_16x16x32_bf16 v[192:195], a[44:47], a[44:47], v[192:195]\n"
"s_sub_u32 s59, s14, 81920\n"
"s_cselect_b32 s14, s58, s59\n"
"v_mfma_f32_16x16x32_bf16 v[252:255], v[196:199], a[44:47], v[252:255]\n"
"s_waitcnt vmcnt(21)\n"
"ds_read_b128 a[48:51], v204 offset:0\n"
"s_add_u32 s27, s12, s14\n"
"ds_read_b128 a[52:55], v204 offset:2048\n"
"s_add_u32 s58, s14, 20480\n"
"ds_read_b128 a[56:59], v205 offset:0\n"
"s_sub_u32 s59, s14, 81920\n"
"s_cselect_b32 s14, s58, s59\n"
"ds_read_b128 a[60:63], v205 offset:2048\n"
"s_waitcnt lgkmcnt(0)\n"
"s_add_u32 m0, s91, 122880\n"
"s_nop 0\n"
"buffer_load_dwordx4 v200, s[8:11], s90 offen offset:0 lds\n"
"s_add_u32 s28, s12, s14\n"
"buffer_load_dwordx4 v201, s[8:11], s90 offen offset:1024 lds\n"
"s_lshl_b32 s58, s49, 5\n"
"buffer_load_dwordx4 v202, s[8:11], s90 offen offset:2048 lds\n"
"v_and_b32 v212, 31, v0\n"
"buffer_load_dwordx4 v203, s[8:11], s90 offen offset:3072 lds\n"
"s_add_u32 s90, s90, 128\n"
"s_cmp_eq_u32 s90, s7\n"
"s_cmov_b32 s90, s3\n"
"v_add_u32 v212, s58, v212\n"
"PAD1_%=:\n"
"s_cbranch_execz PADRET_%=\n"
"s_waitcnt vmcnt(21)\n"
"ds_read_b128 a[64:67], v204 offset:16384\n"
"v_lshlrev_b32 v212, 2, v212\n"
"ds_read_b128 a[68:71], v204 offset:18432\n"
"v_add_u32 v231, 153600, v212\n"
"ds_read_b128 a[72:75], v205 offset:16384\n"
"v_lshrrev_b32 v213, 5, v0\n"
"ds_read_b128 a[76:79], v205 offset:18432\n"
"v_mul_u32_u24 v213, 5120, v213\n"
"v_mfma_f32_16x16x32_bf16 v[188:191], a[48:51], a[48:51], v[188:191]\n"
"v_add_u32 v230, v213, v212\n"
"v_mfma_f32_16x16x32_bf16 v[248:251], v[196:199], a[48:51], v[248:251]\n"
"v_add_u32 v232, 153600, v7\n"
"v_mfma_f32_16x16x32_bf16 v[192:195], a[52:55], a[52:55], v[192:195]\n"
"s_lshl_b32 s88, s48, 3\n"
"v_mfma_f32_16x16x32_bf16 v[252:255], v[196:199], a[52:55], v[252:255]\n"
"s_add_u32 s88, s88, 36864\n"
"v_mfma_f32_16x16x32_bf16 v[188:191], a[56:59], a[56:59], v[188:191]\n"
"v_mfma_f32_16x16x32_bf16 v[248:251], v[196:199], a[56:59], v[248:251]\n"
"v_mfma_f32_16x16x32_bf16 v[192:195], a[60:63], a[60:63], v[192:195]\n"
"v_mfma_f32_16x16x32_bf16 v[252:255], v[196:199], a[60:63], v[252:255]\n"
"s_waitcnt lgkmcnt(3)\n"
"v_mfma_f32_16x16x32_bf16 v[188:191], a[64:67], a[64:67], v[188:191]\n"
"s_waitcnt lgkmcnt(0)\n"
"s_add_u32 m0, s91, 139264\n"
"s_nop 0\n"
"buffer_load_dwordx4 v200, s[8:11], s90 offen offset:0 lds\n"
"v_mfma_f32_16x16x32_bf16 v[248:251], v[196:199], a[64:67], v[248:251]\n"
"v_mfma_f32_16x16x32_bf16 v[192:195], a[68:71], a[68:71], v[192:195]\n"
"buffer_load_dwordx4 v201, s[8:11], s90 offen offset:1024 lds\n"
"v_mfma_f32_16x16x32_bf16 v[252:255], v[196:199], a[68:71], v[252:255]\n"
"v_mfma_f32_16x16x32_bf16 v[188:191], a[72:75], a[72:75], v[188:191]\n"
"buffer_load_dwordx4 v202, s[8:11], s90 offen offset:2048 lds\n"
"v_mfma_f32_16x16x32_bf16 v[248:251], v[196:199], a[72:75], v[248:251]\n"
"v_mfma_f32_16x16x32_bf16 v[192:195], a[76:79], a[76:79], v[192:195]\n"
"buffer_load_dwordx4 v203, s[8:11], s90 offen offset:3072 lds\n"
"s_add_u32 s90, s90, 128\n"
"s_cmp_eq_u32 s90, s7\n"
"s_cmov_b32 s90, s3\n"
"v_mfma_f32_16x16x32_bf16 v[252:255], v[196:199], a[76:79], v[252:255]\n"
"s_waitcnt vmcnt(21)\n"
"ds_read_b128 a[80:83], v204 offset:32768\n"
"ds_read_b128 a[84:87], v204 offset:34816\n"
"ds_read_b128 a[88:91], v205 offset:32768\n"
"ds_read_b128 a[92:95], v205 offset:34816\n"
"s_waitcnt vmcnt(17)\n"
"ds_read_b128 a[96:99], v204 offset:49152\n"
"ds_read_b128 a[100:103], v204 offset:51200\n"
"ds_read_b128 a[104:107], v205 offset:49152\n"
"ds_read_b128 a[108:111], v205 offset:51200\n"
"s_waitcnt lgkmcnt(7)\n"
"v_mfma_f32_16x16x32_bf16 v[188:191], a[80:83], a[80:83], v[188:191]\n"
"v_mfma_f32_16x16x32_bf16 v[248:251], v[196:199], a[80:83], v[248:251]\n"
"s_waitcnt lgkmcnt(6)\n"
"v_mfma_f32_16x16x32_bf16 v[192:195], a[84:87], a[84:87], v[192:195]\n"
"v_mfma_f32_16x16x32_bf16 v[252:255], v[196:199], a[84:87], v[252:255]\n"
"s_waitcnt lgkmcnt(5)\n"
"v_mfma_f32_16x16x32_bf16 v[188:191], a[88:91], a[88:91], v[188:191]\n"
"v_mfma_f32_16x16x32_bf16 v[248:251], v[196:199], a[88:91], v[248:251]\n"
"s_waitcnt lgkmcnt(4)\n"
"v_mfma_f32_16x16x32_bf16 v[192:195], a[92:95], a[92:95], v[192:195]\n"
"v_mfma_f32_16x16x32_bf16 v[252:255], v[196:199], a[92:95], v[252:255]\n"
"s_waitcnt lgkmcnt(3)\n"
"v_mfma_f32_16x16x32_bf16 v[188:191], a[96:99], a[96:99], v[188:191]\n"
"v_mfma_f32_16x16x32_bf16 v[248:251], v[196:199], a[96:99], v[248:251]\n"
"s_waitcnt lgkmcnt(2)\n"
"v_mfma_f32_16x16x32_bf16 v[192:195], a[100:103], a[100:103], v[192:195]\n"
"v_mfma_f32_16x16x32_bf16 v[252:255], v[196:199], a[100:103], v[252:255]\n"
"s_waitcnt lgkmcnt(1)\n"
"v_mfma_f32_16x16x32_bf16 v[188:191], a[104:107], a[104:107], v[188:191]\n"
"v_mfma_f32_16x16x32_bf16 v[248:251], v[196:199], a[104:107], v[248:251]\n"
"s_waitcnt lgkmcnt(0)\n"
"v_mfma_f32_16x16x32_bf16 v[192:195], a[108:111], a[108:111], v[192:195]\n"
"v_mfma_f32_16x16x32_bf16 v[252:255], v[196:199], a[108:111], v[252:255]\n"
"s_waitcnt vmcnt(12)\n"
"s_cmp_lg_u32 s49, 0\n"
"s_nop 4\n"
"s_cbranch_scc1 PS10_%=\n"
"s_mov_b64 exec, 0xffff\n"
"s_mov_b32 s67, 262144\n"
"PL8_%=:\n"
"buffer_load_dwordx2 v[40:41], v10, s[40:43], s15 offen sc1\n"
"s_waitcnt vmcnt(0)\n"
"v_cmp_ne_u64 vcc, s[56:57], v[40:41]\n"
"s_nop 3\n"
"s_cbranch_vccz PD9_%=\n"
"s_sub_u32 s67, s67, 1\n"
"s_cbranch_scc0 PL8_%=\n"
"PD9_%=:\n"
"s_mov_b64 exec, -1\n"
"PS10_%=:\n"
"s_waitcnt lgkmcnt(0)\n"
"s_barrier\n"
"s_add_u32 m0, s13, 40960\n"
"s_nop 0\n"
"buffer_load_dwordx4 v1, s[40:43], s26 offen offset:0 sc1 lds\n"
"buffer_load_dwordx4 v1, s[40:43], s26 offen offset:1024 sc1 lds\n"
"buffer_load_dwordx4 v1, s[40:43], s26 offen offset:2048 sc1 lds\n"
"buffer_load_dwordx4 v1, s[40:43], s26 offen offset:3072 sc1 lds\n"
"s_add_u32 m0, m0, 4096\n"
"s_add_u32 s26, s26, 4096\n"
"buffer_load_dwordx4 v1, s[40:43], s26 offen offset:0 sc1 lds\n"
"s_add_u32 m0, s13, 61440\n"
"s_nop 0\n"
"buffer_load_dwordx4 v1, s[40:43], s27 offen offset:0 sc1 lds\n"
"buffer_load_dwordx4 v1, s[40:43], s27 offen offset:1024 sc1 lds\n"
"buffer_load_dwordx4 v1, s[40:43], s27 offen offset:2048 sc1 lds\n"
"buffer_load_dwordx4 v1, s[40:43], s27 offen offset:3072 sc1 lds\n"
"s_add_u32 m0, m0, 4096\n"
"s_add_u32 s27, s27, 4096\n"
"buffer_load_dwordx4 v1, s[40:43], s27 offen offset:0 sc1 lds\n"
"s_add_u32 s94, s12, 153600\n"
"s_add_u32 s95, s94, 4096\n"
"ds_read_b64_tr_b16 v[136:137], v219 offset:0\n"
"ds_read_b64_tr_b16 v[138:139], v219 offset:512\n"
"ds_read_b64_tr_b16 v[140:141], v219 offset:4096\n"
"ds_read_b64_tr_b16 v[142:143], v219 offset:4608\n"
"ds_read_b64_tr_b16 v[144:145], v219 offset:8192\n"
"ds_read_b64_tr_b16 v[146:147], v219 offset:8704\n"
"ds_read_b64_tr_b16 v[148:149], v219 offset:12288\n"
"ds_read_b64_tr_b16 v[150:151], v219 offset:12800\n"
"ds_read_b64_tr_b16 v[152:153], v219 offset:16384\n"
"ds_read_b64_tr_b16 v[154:155], v219 offset:16896\n"
"s_waitcnt lgkmcnt(8)\n"
"v_mfma_f32_16x16x32_bf16 v[156:159], v[136:139], v[196:199], 0\n"
"s_waitcnt lgkmcnt(6)\n"
"v_mfma_f32_16x16x32_bf16 v[160:163], v[140:143], v[196:199], 0\n"
"s_waitcnt lgkmcnt(4)\n"
"v_mfma_f32_16x16x32_bf16 v[164:167], v[144:147], v[196:199], 0\n"
"s_waitcnt lgkmcnt(2)\n"
"v_mfma_f32_16x16x32_bf16 v[168:171], v[148:151], v[196:199], 0\n"
"s_waitcnt lgkmcnt(0)\n"
"v_mfma_f32_16x16x32_bf16 v[172:175], v[152:155], v[196:199], 0\n"
"s_waitcnt vmcnt(18)\n"
"ds_read_b128 a[112:115], v206 offset:0\n"
"ds_read_b128 a[116:119], v206 offset:2048\n"
"ds_read_b128 a[120:123], v207 offset:0\n"
"ds_read_b128 a[124:127], v207 offset:2048\n"
"s_waitcnt vmcnt(14)\n"
"ds_read_b128 a[128:131], v206 offset:16384\n"
"ds_read_b128 a[132:135], v206 offset:18432\n"
"ds_read_b128 a[136:139], v207 offset:16384\n"
"ds_read_b128 a[140:143], v207 offset:18432\n"
"s_waitcnt vmcnt(10)\n"
"ds_read_b128 a[144:147], v206 offset:32768\n"
"ds_read_b128 a[148:151], v206 offset:34816\n"
"ds_read_b128 a[152:155], v207 offset:32768\n"
"ds_read_b128 a[156:159], v207 offset:34816\n"
"s_waitcnt lgkmcnt(11)\n"
"v_mfma_f32_16x16x32_bf16 v[188:191], a[112:115], a[112:115], v[188:191]\n"
"s_waitcnt lgkmcnt(7)\n"
"v_mfma_f32_16x16x32_bf16 v[188:191], a[128:131], a[128:131], v[188:191]\n"
"buffer_store_dwordx4 v[136:139], v1, s[40:43], s73 offen sc1\n"
"v_mfma_f32_16x16x32_bf16 v[248:251], v[196:199], a[112:115], v[248:251]\n"
"v_mfma_f32_16x16x32_bf16 v[248:251], v[196:199], a[128:131], v[248:251]\n"
"buffer_store_dwordx4 v[140:143], v1, s[40:43], s74 offen sc1\n"
"v_mfma_f32_16x16x32_bf16 v[192:195], a[116:119], a[116:119], v[192:195]\n"
"s_waitcnt lgkmcnt(6)\n"
"v_mfma_f32_16x16x32_bf16 v[192:195], a[132:135], a[132:135], v[192:195]\n"
"buffer_store_dwordx4 v[144:147], v1, s[40:43], s75 offen sc1\n"
"v_mfma_f32_16x16x32_bf16 v[252:255], v[196:199], a[116:119], v[252:255]\n"
"v_mfma_f32_16x16x32_bf16 v[252:255], v[196:199], a[132:135], v[252:255]\n"
"v_mfma_f32_16x16x32_bf16 v[188:191], a[120:123], a[120:123], v[188:191]\n"
"s_waitcnt lgkmcnt(5)\n"
"v_mfma_f32_16x16x32_bf16 v[188:191], a[136:139], a[136:139], v[188:191]\n"
"buffer_store_dwordx4 v[148:151], v1, s[40:43], s76 offen sc1\n"
"v_mfma_f32_16x16x32_bf16 v[248:251], v[196:199], a[120:123], v[248:251]\n"
"v_mfma_f32_16x16x32_bf16 v[248:251], v[196:199], a[136:139], v[248:251]\n"
"buffer_store_dwordx4 v[152:155], v1, s[40:43], s77 offen sc1\n"
"v_mfma_f32_16x16x32_bf16 v[192:195], a[124:127], a[124:127], v[192:195]\n"
"s_waitcnt lgkmcnt(4)\n"
"v_mfma_f32_16x16x32_bf16 v[192:195], a[140:143], a[140:143], v[192:195]\n"
"s_mov_b32 exec_lo, s5\n"
"s_mov_b32 exec_hi, s5\n"
"buffer_store_dwordx4 v[156:159], v7, s[40:43], s4 offen offset:0 sc1\n"
"buffer_store_dwordx4 v[160:163], v7, s[40:43], s4 offen offset:512 sc1\n"
"buffer_store_dwordx4 v[164:167], v7, s[40:43], s4 offen offset:1024 sc1\n"
"buffer_store_dwordx4 v[168:171], v7, s[40:43], s4 offen offset:1536 sc1\n"
"buffer_store_dwordx4 v[172:175], v7, s[40:43], s4 offen offset:2048 sc1\n"
"s_mov_b64 exec, -1\n"
"v_mfma_f32_16x16x32_bf16 v[252:255], v[196:199], a[124:127], v[252:255]\n"
"v_mfma_f32_16x16x32_bf16 v[252:255], v[196:199], a[140:143], v[252:255]\n"
"s_waitcnt lgkmcnt(3)\n"
"v_mfma_f32_16x16x32_bf16 v[188:191], a[144:147], a[144:147], v[188:191]\n"
"v_mfma_f32_16x16x32_bf16 v[248:251], v[196:199], a[144:147], v[248:251]\n"
"s_waitcnt lgkmcnt(2)\n"
"v_mfma_f32_16x16x32_bf16 v[192:195], a[148:151], a[148:151], v[192:195]\n"
"v_mfma_f32_16x16x32_bf16 v[252:255], v[196:199], a[148:151], v[252:255]\n"
"s_waitcnt lgkmcnt(1)\n"
"v_mfma_f32_16x16x32_bf16 v[188:191], a[152:155], a[152:155], v[188:191]\n"
"v_mfma_f32_16x16x32_bf16 v[248:251], v[196:199], a[152:155], v[248:251]\n"
"s_waitcnt lgkmcnt(0)\n"
"v_mfma_f32_16x16x32_bf16 v[192:195], a[156:159], a[156:159], v[192:195]\n"
"v_mfma_f32_16x16x32_bf16 v[252:255], v[196:199], a[156:159], v[252:255]\n"
"s_mov_b32 s78, 0x100001\n"
"s_mov_b32 s79, 0x10000100\n"
"s_mov_b32 s80, 0x200002\n"
"s_mov_b32 s81, 0x20000200\n"
"s_mov_b32 s82, 0x400004\n"
"s_mov_b32 s83, 0x40000400\n"
"s_mov_b32 s84, 0x800008\n"
"s_mov_b32 s85, 0x80000800\n"
"v_cndmask_b32 v220, 0, v188, s[78:79]\n"
"v_cndmask_b32 v222, 0, v192, s[78:79]\n"
"v_cndmask_b32 v220, v220, v189, s[80:81]\n"
"v_cndmask_b32 v222, v222, v193, s[80:81]\n"
"v_cndmask_b32 v220, v220, v190, s[82:83]\n"
"v_cndmask_b32 v222, v222, v194, s[82:83]\n"
"v_cndmask_b32 v220, v220, v191, s[84:85]\n"
"v_cndmask_b32 v222, v222, v195, s[84:85]\n"
"v_mov_b32 v221, v220\n"
"v_mov_b32 v223, v222\n"
"s_nop 0\n"
"v_permlane16_swap_b32 v220, v221\n"
"v_permlane16_swap_b32 v222, v223\n"
"v_add_f32 v220, v220, v221\n"
"v_add_f32 v222, v222, v223\n"
"v_mov_b32 v221, v220\n"
"v_mov_b32 v223, v222\n"
"s_nop 0\n"
"v_permlane32_swap_b32 v220, v221\n"
"v_permlane32_swap_b32 v222, v223\n"
"v_add_f32 v220, v220, v221\n"
"v_add_f32 v222, v222, v223\n"
"v_mul_f32 v16, -0.0015625, v248\n"
"v_mul_f32 v17, -0.0015625, v252\n"
"v_mul_f32 v221, v16, v16\n"
"v_mul_f32 v223, v17, v17\n"
"v_mul_f32 v220, 0.0015625, v220\n"
"v_mul_f32 v222, 0.0015625, v222\n"
"v_sub_f32 v220, v220, v221\n"
"v_sub_f32 v222, v222, v223\n"
"v_max_f32 v220, 0, v220\n"
"v_max_f32 v222, 0, v222\n"
"v_add_f32 v220, 1e-05, v220\n"
"v_add_f32 v222, 1e-05, v222\n"
"v_rsq_f32 v18, v220\n"
"v_rsq_f32 v19, v222\n"
"v_mul_f32 v20, 1.4426950408889634, v18\n"
"v_mul_f32 v21, 1.4426950408889634, v19\n"
"s_waitcnt vmcnt(10)\n"
"s_waitcnt lgkmcnt(0)\n"
"s_barrier\n"
"ds_read_b128 v[96:99], v1 offset:40960\n"
"ds_read_b128 v[100:103], v1 offset:41984\n"
"ds_read_b128 v[104:107], v1 offset:43008\n"
"ds_read_b128 v[108:111], v1 offset:44032\n"
"ds_read_b128 v[112:115], v1 offset:45056\n"
"s_waitcnt lgkmcnt(4)\n"
"v_mfma_f32_16x16x32_bf16 v[56:59], v[96:99], a[0:3], 0\n"
"ds_read_b128 v[116:119], v1 offset:46080\n"
"s_add_u32 s58, s14, 20480\n"
"v_mfma_f32_16x16x32_bf16 v[60:63], v[96:99], a[4:7], 0\n"
"ds_read_b128 v[120:123], v1 offset:47104\n"
"s_sub_u32 s59, s14, 81920\n"
"s_cselect_b32 s14, s58, s59\n"
"s_waitcnt lgkmcnt(5)\n"
"v_mfma_f32_16x16x32_bf16 v[64:67], v[100:103], a[0:3], 0\n"
"ds_read_b128 v[124:127], v1 offset:48128\n"
"s_add_u32 s29, s12, s14\n"
"v_mfma_f32_16x16x32_bf16 v[68:71], v[100:103], a[4:7], 0\n"
"ds_read_b128 v[128:131], v1 offset:49152\n"
"s_add_u32 s58, s14, 20480\n"
"s_waitcnt lgkmcnt(6)\n"
"v_mfma_f32_16x16x32_bf16 v[72:75], v[104:107], a[0:3], 0\n"
"ds_read_b128 v[132:135], v1 offset:50176\n"
"s_sub_u32 s59, s14, 81920\n"
"s_cselect_b32 s14, s58, s59\n"
"v_mfma_f32_16x16x32_bf16 v[76:79], v[104:107], a[4:7], 0\n"
"s_add_u32 m0, s13, 81920\n"
"s_nop 0\n"
"buffer_load_dwordx4 v1, s[40:43], s28 offen offset:0 sc1 lds\n"
"s_waitcnt lgkmcnt(6)\n"
"v_mfma_f32_16x16x32_bf16 v[80:83], v[108:111], a[0:3], 0\n"
"buffer_load_dword v176, v230, s[40:43], s94 offen offset:0 sc1\n"
"s_add_u32 s30, s12, s14\n"
"v_mfma_f32_16x16x32_bf16 v[84:87], v[108:111], a[4:7], 0\n"
"buffer_load_dwordx4 v1, s[40:43], s28 offen offset:1024 sc1 lds\n"
"s_waitcnt lgkmcnt(5)\n"
"v_mfma_f32_16x16x32_bf16 v[88:91], v[112:115], a[0:3], 0\n"
"s_lshr_b32 s60, s49, 1\n"
"v_mfma_f32_16x16x32_bf16 v[92:95], v[112:115], a[4:7], 0\n"
"buffer_load_dwordx4 v1, s[40:43], s28 offen offset:2048 sc1 lds\n"
"s_waitcnt lgkmcnt(4)\n"
"v_mfma_f32_16x16x32_bf16 v[56:59], v[116:119], a[8:11], v[56:59]\n"
"ds_read_b128 v[96:99], v1 offset:51200\n"
"s_mul_i32 s86, s60, 5120\n"
"v_mfma_f32_16x16x32_bf16 v[60:63], v[116:119], a[12:15], v[60:63]\n"
"ds_read_b128 v[100:103], v1 offset:52224\n"
"s_and_b32 s61, s49, 1\n"
"s_waitcnt lgkmcnt(5)\n"
"v_mfma_f32_16x16x32_bf16 v[64:67], v[120:123], a[8:11], v[64:67]\n"
"ds_read_b128 v[104:107], v1 offset:53248\n"
"s_lshl_b32 s61, s61, 11\n"
"v_mfma_f32_16x16x32_bf16 v[68:71], v[120:123], a[12:15], v[68:71]\n"
"ds_read_b128 v[108:111], v1 offset:54272\n"
"s_add_u32 s86, s86, s61\n"
"s_waitcnt lgkmcnt(6)\n"
"v_mfma_f32_16x16x32_bf16 v[72:75], v[124:127], a[8:11], v[72:75]\n"
"ds_read_b128 v[112:115], v1 offset:55296\n"
"s_add_u32 s62, s12, 102400\n"
"v_mfma_f32_16x16x32_bf16 v[76:79], v[124:127], a[12:15], v[76:79]\n"
"buffer_load_dwordx4 v1, s[40:43], s28 offen offset:3072 sc1 lds\n"
"s_waitcnt lgkmcnt(6)\n"
"v_mfma_f32_16x16x32_bf16 v[80:83], v[128:131], a[8:11], v[80:83]\n"
"buffer_load_dword v177, v230, s[40:43], s94 offen offset:512 sc1\n"
"s_add_u32 s58, s50, s49\n"
"v_mfma_f32_16x16x32_bf16 v[84:87], v[128:131], a[12:15], v[84:87]\n"
"s_add_u32 m0, m0, 4096\n"
"s_add_u32 s28, s28, 4096\n"
"buffer_load_dwordx4 v1, s[40:43], s28 offen offset:0 sc1 lds\n"
"s_waitcnt lgkmcnt(5)\n"
"v_mfma_f32_16x16x32_bf16 v[88:91], v[132:135], a[8:11], v[88:91]\n"
"s_sub_u32 s59, s58, 10\n"
"s_cselect_b32 s58, s58, s59\n"
"v_mfma_f32_16x16x32_bf16 v[92:95], v[132:135], a[12:15], v[92:95]\n"
"s_add_u32 m0, s13, 0\n"
"s_nop 0\n"
"buffer_load_dwordx4 v1, s[40:43], s29 offen offset:0 sc1 lds\n"
"s_waitcnt lgkmcnt(4)\n"
"v_mfma_f32_16x16x32_bf16 v[56:59], v[96:99], a[16:19], v[56:59]\n"
"ds_read_b128 v[116:119], v1 offset:56320\n"
"s_mul_i32 s58, s58, 5120\n"
"v_mfma_f32_16x16x32_bf16 v[60:63], v[96:99], a[20:23], v[60:63]\n"
"ds_read_b128 v[120:123], v1 offset:57344\n"
"s_add_u32 s31, s58, s62\n"
"s_waitcnt lgkmcnt(5)\n"
"v_mfma_f32_16x16x32_bf16 v[64:67], v[100:103], a[16:19], v[64:67]\n"
"ds_read_b128 v[124:127], v1 offset:58368\n"
"s_add_u32 s58, s50, s49\n"
"v_mfma_f32_16x16x32_bf16 v[68:71], v[100:103], a[20:23], v[68:71]\n"
"ds_read_b128 v[128:131], v1 offset:59392\n"
"s_add_u32 s58, s58, 4\n"
"s_waitcnt lgkmcnt(6)\n"
"v_mfma_f32_16x16x32_bf16 v[72:75], v[104:107], a[16:19], v[72:75]\n"
"ds_read_b128 v[132:135], v1 offset:60416\n"
"s_sub_u32 s59, s58, 10\n"
"s_cselect_b32 s58, s58, s59\n"
"v_mfma_f32_16x16x32_bf16 v[76:79], v[104:107], a[20:23], v[76:79]\n"
"buffer_load_dwordx4 v1, s[40:43], s29 offen offset:1024 sc1 lds\n"
"s_waitcnt lgkmcnt(6)\n"
"v_mfma_f32_16x16x32_bf16 v[80:83], v[108:111], a[16:19], v[80:83]\n"
"buffer_load_dword v178, v230, s[40:43], s94 offen offset:1024 sc1\n"
"s_mul_i32 s58, s58, 5120\n"
"v_mfma_f32_16x16x32_bf16 v[84:87], v[108:111], a[20:23], v[84:87]\n"
"buffer_load_dwordx4 v1, s[40:43], s29 offen offset:2048 sc1 lds\n"
"s_waitcnt lgkmcnt(5)\n"
"v_mfma_f32_16x16x32_bf16 v[88:91], v[112:115], a[16:19], v[88:91]\n"
"s_add_u32 s89, s58, s62\n"
"v_mfma_f32_16x16x32_bf16 v[92:95], v[112:115], a[20:23], v[92:95]\n"
"buffer_load_dwordx4 v1, s[40:43], s29 offen offset:3072 sc1 lds\n"
"s_waitcnt vmcnt(17)\n"
"s_waitcnt vmcnt(12)\n"
"s_waitcnt lgkmcnt(0)\n"
"s_barrier\n"
"s_cmp_lg_u32 s49, 1\n"
"s_nop 0\n"
"s_cbranch_scc1 FS11_%=\n"
"s_mov_b64 exec, 1\n"
"buffer_store_dwordx2 v[4:5], off, s[40:43], s88 sc1\n"
"s_mov_b64 exec, -1\n"
"FS11_%=:\n"
"v_mfma_f32_16x16x32_bf16 v[56:59], v[116:119], a[24:27], v[56:59]\n"
"ds_read_b128 v[96:99], v1 offset:61440\n"
"s_add_u32 s58, s50, s60\n"
"v_mfma_f32_16x16x32_bf16 v[60:63], v[116:119], a[28:31], v[60:63]\n"
"ds_read_b128 v[100:103], v1 offset:62464\n"
"s_add_u32 s58, s58, 8\n"
"v_mfma_f32_16x16x32_bf16 v[64:67], v[120:123], a[24:27], v[64:67]\n"
"ds_read_b128 v[104:107], v1 offset:63488\n"
"s_sub_u32 s59, s58, 10\n"
"s_cselect_b32 s58, s58, s59\n"
"v_mfma_f32_16x16x32_bf16 v[68:71], v[120:123], a[28:31], v[68:71]\n"
"ds_read_b128 v[108:111], v1 offset:64512\n"
"s_mul_i32 s58, s58, 5120\n"
"v_mfma_f32_16x16x32_bf16 v[72:75], v[124:127], a[24:27], v[72:75]\n"
"ds_read_b128 v[112:115], v2 offset:0\n"
"s_add_u32 s87, s58, s62\n"
"v_mfma_f32_16x16x32_bf16 v[76:79], v[124:127], a[28:31], v[76:79]\n"
"s_add_u32 m0, m0, 4096\n"
"s_add_u32 s29, s29, 4096\n"
"buffer_load_dwordx4 v1, s[40:43], s29 offen offset:0 sc1 lds\n"
"v_mfma_f32_16x16x32_bf16 v[80:83], v[128:131], a[24:27], v[80:83]\n"
"buffer_load_dword v179, v230, s[40:43], s94 offen offset:1536 sc1\n"
"s_add_u32 s87, s87, s61\n"
"v_mfma_f32_16x16x32_bf16 v[84:87], v[128:131], a[28:31], v[84:87]\n"
"s_and_b32 s20, s55, 7\n"
"v_mfma_f32_16x16x32_bf16 v[88:91], v[132:135], a[24:27], v[88:91]\n"
"s_mul_i32 s6, s20, 102400\n"
"v_mfma_f32_16x16x32_bf16 v[92:95], v[132:135], a[28:31], v[92:95]\n"
"s_mul_i32 s58, s49, 25600\n"
"s_waitcnt lgkmcnt(4)\n"
"v_mfma_f32_16x16x32_bf16 v[56:59], v[96:99], a[32:35], v[56:59]\n"
"ds_read_b128 v[116:119], v2 offset:1024\n"
"s_add_u32 s6, s6, s58\n"
"v_mfma_f32_16x16x32_bf16 v[60:63], v[96:99], a[36:39], v[60:63]\n"
"ds_read_b128 v[120:123], v2 offset:2048\n"
"s_add_u32 s6, s6, 2883584\n"
"s_waitcnt lgkmcnt(5)\n"
"v_mfma_f32_16x16x32_bf16 v[64:67], v[100:103], a[32:35], v[64:67]\n"
"ds_read_b128 v[124:127], v2 offset:3072\n"
"s_lshr_b32 s25, s55, 3\n"
"v_mfma_f32_16x16x32_bf16 v[68:71], v[100:103], a[36:39], v[68:71]\n"
"ds_read_b128 v[128:131], v2 offset:4096\n"
"s_lshl_b32 s58, s54, 3\n"
"s_waitcnt lgkmcnt(6)\n"
"v_mfma_f32_16x16x32_bf16 v[72:75], v[104:107], a[32:35], v[72:75]\n"
"ds_read_b128 v[132:135], v2 offset:5120\n"
"s_lshl_b32 s59, s25, 2\n"
"v_mfma_f32_16x16x32_bf16 v[76:79], v[104:107], a[36:39], v[76:79]\n"
"s_add_u32 m0, s13, 20480\n"
"s_nop 0\n"
"buffer_load_dwordx4 v1, s[40:43], s30 offen offset:0 sc1 lds\n"
"s_waitcnt lgkmcnt(6)\n"
"v_mfma_f32_16x16x32_bf16 v[80:83], v[108:111], a[32:35], v[80:83]\n"
"buffer_load_dword v180, v230, s[40:43], s94 offen offset:2048 sc1\n"
"s_add_u32 s58, s58, s59\n"
"v_mfma_f32_16x16x32_bf16 v[84:87], v[108:111], a[36:39], v[84:87]\n"
"buffer_load_dwordx4 v1, s[40:43], s30 offen offset:1024 sc1 lds\n"
"s_waitcnt lgkmcnt(5)\n"
"v_mfma_f32_16x16x32_bf16 v[88:91], v[112:115], a[32:35], v[88:91]\n"
"s_add_u32 s24, s58, s49\n"
"v_mfma_f32_16x16x32_bf16 v[92:95], v[112:115], a[36:39], v[92:95]\n"
"buffer_load_dwordx4 v1, s[40:43], s30 offen offset:2048 sc1 lds\n"
"s_waitcnt lgkmcnt(4)\n"
"v_mfma_f32_16x16x32_bf16 v[56:59], v[116:119], a[40:43], v[56:59]\n"
"ds_read_b128 v[96:99], v2 offset:6144\n"
"s_mul_i32 s23, s24, 20480\n"
"v_mfma_f32_16x16x32_bf16 v[60:63], v[116:119], a[44:47], v[60:63]\n"
"ds_read_b128 v[100:103], v2 offset:7168\n"
"s_mul_i32 s59, s20, 160\n"
"s_waitcnt lgkmcnt(5)\n"
"v_mfma_f32_16x16x32_bf16 v[64:67], v[120:123], a[40:43], v[64:67]\n"
"ds_read_b128 v[104:107], v2 offset:8192\n"
"s_add_u32 s23, s23, s59\n"
"v_mfma_f32_16x16x32_bf16 v[68:71], v[120:123], a[44:47], v[68:71]\n"
"ds_read_b128 v[108:111], v2 offset:9216\n"
"v_mul_u32_u24 v11, 1280, v8\n"
"s_waitcnt lgkmcnt(6)\n"
"v_mfma_f32_16x16x32_bf16 v[72:75], v[124:127], a[40:43], v[72:75]\n"
"ds_read_b128 v[112:115], v2 offset:10240\n"
"v_lshl_add_u32 v11, v9, 3, v11\n"
"v_mfma_f32_16x16x32_bf16 v[76:79], v[124:127], a[44:47], v[76:79]\n"
"buffer_load_dwordx4 v1, s[40:43], s30 offen offset:3072 sc1 lds\n"
"s_waitcnt lgkmcnt(6)\n"
"v_mfma_f32_16x16x32_bf16 v[80:83], v[128:131], a[40:43], v[80:83]\n"
"buffer_load_dword v181, v230, s[40:43], s94 offen offset:2560 sc1\n"
"s_lshr_b32 s62, s20, 1\n"
"v_mfma_f32_16x16x32_bf16 v[84:87], v[128:131], a[44:47], v[84:87]\n"
"s_add_u32 m0, m0, 4096\n"
"s_add_u32 s30, s30, 4096\n"
"buffer_load_dwordx4 v1, s[40:43], s30 offen offset:0 sc1 lds\n"
"s_waitcnt lgkmcnt(5)\n"
"v_mfma_f32_16x16x32_bf16 v[88:91], v[132:135], a[40:43], v[88:91]\n"
"s_and_b32 s63, s20, 1\n"
"v_mfma_f32_16x16x32_bf16 v[92:95], v[132:135], a[44:47], v[92:95]\n"
"s_add_u32 m0, s13, 102400\n"
"s_nop 0\n"
"buffer_load_dwordx4 v1, s[40:43], s31 offen offset:0 sc1 lds\n"
"s_waitcnt lgkmcnt(4)\n"
"v_mfma_f32_16x16x32_bf16 v[56:59], v[96:99], a[48:51], v[56:59]\n"
"ds_read_b128 v[116:119], v2 offset:11264\n"
"s_lshl_b32 s64, s24, 2\n"
"v_mfma_f32_16x16x32_bf16 v[60:63], v[96:99], a[52:55], v[60:63]\n"
"ds_read_b128 v[120:123], v2 offset:12288\n"
"s_add_u32 s64, s64, s62\n"
"s_waitcnt lgkmcnt(5)\n"
"v_mfma_f32_16x16x32_bf16 v[64:67], v[100:103], a[48:51], v[64:67]\n"
"ds_read_b128 v[124:127], v2 offset:13312\n"
"s_mul_i32 s64, s64, 5120\n"
"v_mfma_f32_16x16x32_bf16 v[68:71], v[100:103], a[52:55], v[68:71]\n"
"ds_read_b128 v[128:131], v2 offset:14336\n"
"s_lshl_b32 s63, s63, 11\n"
"s_waitcnt lgkmcnt(6)\n"
"v_mfma_f32_16x16x32_bf16 v[72:75], v[104:107], a[48:51], v[72:75]\n"
"ds_read_b128 v[132:135], v2 offset:15360\n"
"s_add_u32 s64, s64, s63\n"
"v_mfma_f32_16x16x32_bf16 v[76:79], v[104:107], a[52:55], v[76:79]\n"
"buffer_load_dwordx4 v1, s[40:43], s31 offen offset:1024 sc1 lds\n"
"s_waitcnt lgkmcnt(6)\n"
"v_mfma_f32_16x16x32_bf16 v[80:83], v[108:111], a[48:51], v[80:83]\n"
"buffer_load_dword v182, v230, s[40:43], s94 offen offset:3072 sc1\n"
"s_add_u32 s69, s64, 11980800\n"
"v_mfma_f32_16x16x32_bf16 v[84:87], v[108:111], a[52:55], v[84:87]\n"
"buffer_load_dwordx4 v1, s[40:43], s31 offen offset:2048 sc1 lds\n"
"s_waitcnt lgkmcnt(5)\n"
"v_mfma_f32_16x16x32_bf16 v[88:91], v[112:115], a[48:51], v[88:91]\n"
"s_and_b32 s62, s53, 7\n"
"v_mfma_f32_16x16x32_bf16 v[92:95], v[112:115], a[52:55], v[92:95]\n"
"buffer_load_dwordx4 v1, s[40:43], s31 offen offset:3072 sc1 lds\n"
"s_waitcnt vmcnt(19)\n"
"s_barrier\n"
"s_waitcnt lgkmcnt(4)\n"
"v_mfma_f32_16x16x32_bf16 v[56:59], v[116:119], a[56:59], v[56:59]\n"
"ds_read_b128 v[96:99], v2 offset:16384\n"
"s_mul_i32 s63, s62, 7\n"
"v_mfma_f32_16x16x32_bf16 v[60:63], v[116:119], a[60:63], v[60:63]\n"
"ds_read_b128 v[100:103], v2 offset:17408\n"
"s_min_u32 s63, s63, 48\n"
"s_waitcnt lgkmcnt(5)\n"
"v_mfma_f32_16x16x32_bf16 v[64:67], v[120:123], a[56:59], v[64:67]\n"
"ds_read_b128 v[104:107], v2 offset:18432\n"
"s_add_u32 s63, s63, 1\n"
"v_mfma_f32_16x16x32_bf16 v[68:71], v[120:123], a[60:63], v[68:71]\n"
"ds_read_b128 v[108:111], v2 offset:19456\n"
"s_lshr_b32 s63, s63, 1\n"
"s_waitcnt lgkmcnt(6)\n"
"v_mfma_f32_16x16x32_bf16 v[72:75], v[124:127], a[56:59], v[72:75]\n"
"ds_read_b128 v[112:115], v2 offset:20480\n"
"s_lshr_b32 s65, s53, 3\n"
"v_mfma_f32_16x16x32_bf16 v[76:79], v[124:127], a[60:63], v[76:79]\n"
"s_add_u32 m0, m0, 4096\n"
"s_add_u32 s31, s31, 4096\n"
"buffer_load_dwordx4 v1, s[40:43], s31 offen offset:0 sc1 lds\n"
"s_waitcnt lgkmcnt(6)\n"
"v_mfma_f32_16x16x32_bf16 v[80:83], v[128:131], a[56:59], v[80:83]\n"
"buffer_load_dword v183, v230, s[40:43], s94 offen offset:3584 sc1\n"
"s_mul_i32 s65, s65, 27\n"
"v_mfma_f32_16x16x32_bf16 v[84:87], v[128:131], a[60:63], v[84:87]\n"
"s_add_u32 s63, s63, s65\n"
"s_waitcnt lgkmcnt(5)\n"
"v_mfma_f32_16x16x32_bf16 v[88:91], v[132:135], a[56:59], v[88:91]\n"
"s_mul_i32 s65, s54, 432\n"
"v_mfma_f32_16x16x32_bf16 v[92:95], v[132:135], a[60:63], v[92:95]\n"
"s_add_u32 m0, s13, 122880\n"
"s_nop 0\n"
"buffer_load_dwordx4 v1, s[40:43], s89 offen offset:0 sc1 lds\n"
"s_waitcnt lgkmcnt(4)\n"
"v_mfma_f32_16x16x32_bf16 v[56:59], v[96:99], a[64:67], v[56:59]\n"
"ds_read_b128 v[116:119], v2 offset:21504\n"
"s_add_u32 s63, s63, s65\n"
"v_mfma_f32_16x16x32_bf16 v[60:63], v[96:99], a[68:71], v[60:63]\n"
"ds_read_b128 v[120:123], v2 offset:22528\n"
"s_lshl_b32 s63, s63, 10\n"
"s_waitcnt lgkmcnt(5)\n"
"v_mfma_f32_16x16x32_bf16 v[64:67], v[100:103], a[64:67], v[64:67]\n"
"ds_read_b128 v[124:127], v2 offset:23552\n"
"s_add_u32 s70, s63, 15065088\n"
"v_mfma_f32_16x16x32_bf16 v[68:71], v[100:103], a[68:71], v[68:71]\n"
"ds_read_b128 v[128:131], v2 offset:24576\n"
"s_lshr_b32 s62, s49, 1\n"
"s_waitcnt lgkmcnt(6)\n"
"v_mfma_f32_16x16x32_bf16 v[72:75], v[104:107], a[64:67], v[72:75]\n"
"ds_read_b128 v[132:135], v2 offset:25600\n"
"s_mul_i32 s62, s62, 27\n"
"v_mfma_f32_16x16x32_bf16 v[76:79], v[104:107], a[68:71], v[76:79]\n"
"buffer_load_dwordx4 v1, s[40:43], s89 offen offset:1024 sc1 lds\n"
"s_waitcnt lgkmcnt(6)\n"
"v_mfma_f32_16x16x32_bf16 v[80:83], v[108:111], a[64:67], v[80:83]\n"
"buffer_load_dword v184, v230, s[40:43], s95 offen offset:0 sc1\n"
"s_and_b32 s63, s49, 1\n"
"v_mfma_f32_16x16x32_bf16 v[84:87], v[108:111], a[68:71], v[84:87]\n"
"s_mul_i32 s64, s63, 14\n"
"s_waitcnt lgkmcnt(5)\n"
"v_mfma_f32_16x16x32_bf16 v[88:91], v[112:115], a[64:67], v[88:91]\n"
"s_add_u32 s22, s62, s64\n"
"v_mfma_f32_16x16x32_bf16 v[92:95], v[112:115], a[68:71], v[92:95]\n"
"buffer_load_dwordx4 v1, s[40:43], s89 offen offset:2048 sc1 lds\n"
"s_waitcnt lgkmcnt(4)\n"
"v_mfma_f32_16x16x32_bf16 v[56:59], v[116:119], a[72:75], v[56:59]\n"
"ds_read_b128 v[96:99], v2 offset:26624\n"
"s_mul_i32 s62, s20, 54\n"
"v_mfma_f32_16x16x32_bf16 v[60:63], v[116:119], a[76:79], v[60:63]\n"
"ds_read_b128 v[100:103], v2 offset:27648\n"
"s_add_u32 s62, s62, s22\n"
"s_waitcnt lgkmcnt(5)\n"
"v_mfma_f32_16x16x32_bf16 v[64:67], v[120:123], a[72:75], v[64:67]\n"
"ds_read_b128 v[104:107], v2 offset:28672\n"
"s_mul_i32 s62, s62, 5120\n"
"v_mfma_f32_16x16x32_bf16 v[68:71], v[120:123], a[76:79], v[68:71]\n"
"ds_read_b128 v[108:111], v2 offset:29696\n"
"s_add_u32 s21, s62, 6160384\n"
"s_waitcnt lgkmcnt(6)\n"
"v_mfma_f32_16x16x32_bf16 v[72:75], v[124:127], a[72:75], v[72:75]\n"
"ds_read_b128 v[112:115], v2 offset:30720\n"
"s_mul_i32 s63, s63, 5120\n"
"v_mfma_f32_16x16x32_bf16 v[76:79], v[124:127], a[76:79], v[76:79]\n"
"buffer_load_dwordx4 v1, s[40:43], s89 offen offset:3072 sc1 lds\n"
"s_waitcnt lgkmcnt(6)\n"
"v_mfma_f32_16x16x32_bf16 v[80:83], v[128:131], a[72:75], v[80:83]\n"
"buffer_load_dword v185, v230, s[40:43], s95 offen offset:512 sc1\n"
"s_sub_u32 s47, s21, s63\n"
"v_mfma_f32_16x16x32_bf16 v[84:87], v[128:131], a[76:79], v[84:87]\n"
"s_lshl_b32 s15, s53, 11\n"
"s_waitcnt lgkmcnt(5)\n"
"v_mfma_f32_16x16x32_bf16 v[88:91], v[132:135], a[72:75], v[88:91]\n"
"s_add_u32 s15, s15, 69632\n"
"v_mfma_f32_16x16x32_bf16 v[92:95], v[132:135], a[76:79], v[92:95]\n"
"s_add_u32 m0, m0, 4096\n"
"s_add_u32 s89, s89, 4096\n"
"buffer_load_dwordx4 v1, s[40:43], s89 offen offset:0 sc1 lds\n"
"s_waitcnt lgkmcnt(4)\n"
"v_mfma_f32_16x16x32_bf16 v[56:59], v[96:99], a[80:83], v[56:59]\n"
"ds_read_b128 v[116:119], v2 offset:31744\n"
"v_lshrrev_b32 v208, 1, v9\n"
"v_mfma_f32_16x16x32_bf16 v[60:63], v[96:99], a[84:87], v[60:63]\n"
"ds_read_b128 v[120:123], v2 offset:32768\n"
"v_and_b32 v209, 1, v9\n"
"s_waitcnt lgkmcnt(5)\n"
"v_mfma_f32_16x16x32_bf16 v[64:67], v[100:103], a[80:83], v[64:67]\n"
"ds_read_b128 v[124:127], v2 offset:33792\n"
"s_lshl_b32 s58, s49, 5\n"
"v_mfma_f32_16x16x32_bf16 v[68:71], v[100:103], a[84:87], v[68:71]\n"
"ds_read_b128 v[128:131], v2 offset:34816\n"
"v_add_u32 v210, s58, v8\n"
"s_waitcnt lgkmcnt(6)\n"
"v_mfma_f32_16x16x32_bf16 v[72:75], v[104:107], a[80:83], v[72:75]\n"
"ds_read_b128 v[132:135], v2 offset:35840\n"
"v_mul_u32_u24 v210, 288, v210\n"
"v_mfma_f32_16x16x32_bf16 v[76:79], v[104:107], a[84:87], v[76:79]\n"
"s_add_u32 m0, s86, 143360\n"
"s_nop 0\n"
"buffer_load_dwordx4 v1, s[40:43], s87 offen offset:0 sc1 lds\n"
"s_waitcnt lgkmcnt(6)\n"
"v_mfma_f32_16x16x32_bf16 v[80:83], v[108:111], a[80:83], v[80:83]\n"
"v_add_u32 v210, 40960, v210\n"
"v_mfma_f32_16x16x32_bf16 v[84:87], v[108:111], a[84:87], v[84:87]\n"
"v_mul_u32_u24 v211, 80, v208\n"
"s_waitcnt lgkmcnt(5)\n"
"v_mfma_f32_16x16x32_bf16 v[88:91], v[112:115], a[80:83], v[88:91]\n"
"PAD2_%=:\n"
"s_cbranch_execz PADRET_%=\n"
"v_add_u32 v211, v211, v210\n"
"v_mfma_f32_16x16x32_bf16 v[92:95], v[112:115], a[84:87], v[92:95]\n"
"buffer_load_dwordx4 v1, s[40:43], s87 offen offset:1024 sc1 lds\n"
"s_waitcnt vmcnt(24)\n"
"s_barrier\n"
"s_waitcnt lgkmcnt(4)\n"
"v_mfma_f32_16x16x32_bf16 v[56:59], v[116:119], a[88:91], v[56:59]\n"
"ds_read_b128 v[96:99], v1 offset:0\n"
"v_lshl_add_u32 v224, v209, 4, v211\n"
"v_mfma_f32_16x16x32_bf16 v[60:63], v[116:119], a[92:95], v[60:63]\n"
"ds_read_b128 v[100:103], v1 offset:1024\n"
"v_add_f32 v176, v176, v177\n"
"s_waitcnt lgkmcnt(5)\n"
"v_mfma_f32_16x16x32_bf16 v[64:67], v[120:123], a[88:91], v[64:67]\n"
"ds_read_b128 v[104:107], v1 offset:2048\n"
"v_lshl_add_u32 v225, v209, 3, v211\n"
"v_mfma_f32_16x16x32_bf16 v[68:71], v[120:123], a[92:95], v[68:71]\n"
"ds_read_b128 v[108:111], v1 offset:3072\n"
"v_add_f32 v176, v176, v178\n"
"s_waitcnt lgkmcnt(6)\n"
"v_mfma_f32_16x16x32_bf16 v[72:75], v[124:127], a[88:91], v[72:75]\n"
"ds_read_b128 v[112:115], v1 offset:4096\n"
"v_add_u32 v225, 64, v225\n"
"v_mfma_f32_16x16x32_bf16 v[76:79], v[124:127], a[92:95], v[76:79]\n"
"buffer_load_dwordx4 v1, s[40:43], s87 offen offset:2048 sc1 lds\n"
"s_waitcnt lgkmcnt(6)\n"
"v_mfma_f32_16x16x32_bf16 v[80:83], v[128:131], a[88:91], v[80:83]\n"
"s_waitcnt vmcnt(24)\n"
"v_add_f32 v176, v176, v179\n"
"v_mfma_f32_16x16x32_bf16 v[84:87], v[128:131], a[92:95], v[84:87]\n"
"v_mul_u32_u24 v211, 24, v9\n"
"s_waitcnt lgkmcnt(5)\n"
"v_mfma_f32_16x16x32_bf16 v[88:91], v[132:135], a[88:91], v[88:91]\n"
"s_waitcnt vmcnt(22)\n"
"v_add_f32 v176, v176, v180\n"
"v_mfma_f32_16x16x32_bf16 v[92:95], v[132:135], a[92:95], v[92:95]\n"
"buffer_load_dwordx2 v[238:239], v11, s[8:11], s23 offen offset:0\n"
"s_waitcnt lgkmcnt(4)\n"
"v_mfma_f32_16x16x32_bf16 v[56:59], v[96:99], a[96:99], v[56:59]\n"
"ds_read_b128 v[116:119], v1 offset:5120\n"
"v_add_u32 v211, v211, v210\n"
"v_mfma_f32_16x16x32_bf16 v[60:63], v[96:99], a[100:103], v[60:63]\n"
"ds_read_b128 v[120:123], v1 offset:6144\n"
"s_waitcnt vmcnt(19)\n"
"v_add_f32 v176, v176, v181\n"
"s_waitcnt lgkmcnt(5)\n"
"v_mfma_f32_16x16x32_bf16 v[64:67], v[100:103], a[96:99], v[64:67]\n"
"ds_read_b128 v[124:127], v1 offset:7168\n"
"v_add_u32 v226, 160, v211\n"
"v_mfma_f32_16x16x32_bf16 v[68:71], v[100:103], a[100:103], v[68:71]\n"
"ds_read_b128 v[128:131], v1 offset:8192\n"
"s_waitcnt vmcnt(15)\n"
"v_add_f32 v176, v176, v182\n"
"s_waitcnt lgkmcnt(6)\n"
"v_mfma_f32_16x16x32_bf16 v[72:75], v[104:107], a[96:99], v[72:75]\n"
"ds_read_b128 v[132:135], v1 offset:9216\n"
"v_lshrrev_b32 v210, 2, v8\n"
"v_mfma_f32_16x16x32_bf16 v[76:79], v[104:107], a[100:103], v[76:79]\n"
"buffer_load_dwordx2 v[240:241], v11, s[8:11], s23 offen offset:32\n"
"s_waitcnt lgkmcnt(6)\n"
"v_mfma_f32_16x16x32_bf16 v[80:83], v[108:111], a[96:99], v[80:83]\n"
"s_waitcnt vmcnt(12)\n"
"v_add_f32 v176, v176, v183\n"
"v_mfma_f32_16x16x32_bf16 v[84:87], v[108:111], a[100:103], v[84:87]\n"
"v_and_b32 v211, 3, v8\n"
"s_waitcnt lgkmcnt(5)\n"
"v_mfma_f32_16x16x32_bf16 v[88:91], v[112:115], a[96:99], v[88:91]\n"
"s_waitcnt vmcnt(9)\n"
"v_add_f32 v176, v176, v184\n"
"v_mfma_f32_16x16x32_bf16 v[92:95], v[112:115], a[100:103], v[92:95]\n"
"buffer_load_dwordx2 v[242:243], v11, s[8:11], s23 offen offset:64\n"
"s_waitcnt lgkmcnt(4)\n"
"v_mfma_f32_16x16x32_bf16 v[56:59], v[116:119], a[104:107], v[56:59]\n"
"ds_read_b128 v[96:99], v1 offset:10240\n"
"v_lshl_add_u32 v212, v9, 2, v210\n"
"v_mfma_f32_16x16x32_bf16 v[60:63], v[116:119], a[108:111], v[60:63]\n"
"ds_read_b128 v[100:103], v1 offset:11264\n"
"s_waitcnt vmcnt(7)\n"
"v_add_f32 v176, v176, v185\n"
"s_waitcnt lgkmcnt(5)\n"
"v_mfma_f32_16x16x32_bf16 v[64:67], v[120:123], a[104:107], v[64:67]\n"
"ds_read_b128 v[104:107], v1 offset:12288\n"
"v_mul_u32_u24 v212, 288, v212\n"
"v_mfma_f32_16x16x32_bf16 v[68:71], v[120:123], a[108:111], v[68:71]\n"
"ds_read_b128 v[108:111], v1 offset:13312\n"
"v_mov_b32 v177, v176\n"
"s_waitcnt lgkmcnt(6)\n"
"v_mfma_f32_16x16x32_bf16 v[72:75], v[124:127], a[104:107], v[72:75]\n"
"ds_read_b128 v[112:115], v1 offset:14336\n"
"v_lshl_add_u32 v212, v211, 3, v212\n"
"v_mfma_f32_16x16x32_bf16 v[76:79], v[124:127], a[108:111], v[76:79]\n"
"buffer_load_dwordx2 v[244:245], v11, s[8:11], s23 offen offset:96\n"
"s_waitcnt lgkmcnt(6)\n"
"v_mfma_f32_16x16x32_bf16 v[80:83], v[128:131], a[104:107], v[80:83]\n"
"v_permlane32_swap_b32 v176, v177\n"
"v_mfma_f32_16x16x32_bf16 v[84:87], v[128:131], a[108:111], v[84:87]\n"
"v_add_u32 v212, 40960, v212\n"
"s_waitcnt lgkmcnt(5)\n"
"v_mfma_f32_16x16x32_bf16 v[88:91], v[132:135], a[104:107], v[88:91]\n"
"v_add_f32 v176, v176, v177\n"
"ds_write_b32 v231, v176\n"
"v_mfma_f32_16x16x32_bf16 v[92:95], v[132:135], a[108:111], v[92:95]\n"
"buffer_load_dwordx2 v[246:247], v11, s[8:11], s23 offen offset:128\n"
"s_waitcnt lgkmcnt(5)\n"
"v_mfma_f32_16x16x32_bf16 v[56:59], v[96:99], a[112:115], v[56:59]\n"
"ds_read_b128 v[116:119], v1 offset:15360\n"
"v_add_u32 v227, s58, v212\n"
"v_mfma_f32_16x16x32_bf16 v[60:63], v[96:99], a[116:119], v[60:63]\n"
"ds_read_b128 v[120:123], v1 offset:16384\n"
"s_lshr_b32 s59, s49, 1\n"
"s_waitcnt lgkmcnt(6)\n"
"v_mfma_f32_16x16x32_bf16 v[64:67], v[100:103], a[112:115], v[64:67]\n"
"ds_read_b128 v[124:127], v1 offset:17408\n"
"s_lshl_b32 s60, s59, 5\n"
"v_mfma_f32_16x16x32_bf16 v[68:71], v[100:103], a[116:119], v[68:71]\n"
"ds_read_b128 v[128:131], v1 offset:18432\n"
"s_add_u32 s60, s60, 160\n"
"s_waitcnt lgkmcnt(7)\n"
"v_mfma_f32_16x16x32_bf16 v[72:75], v[104:107], a[112:115], v[72:75]\n"
"ds_read_b128 v[132:135], v1 offset:19456\n"
"v_add_u32 v228, s60, v212\n"
"v_mfma_f32_16x16x32_bf16 v[76:79], v[104:107], a[116:119], v[76:79]\n"
"s_cmp_eq_u32 s49, 3\n"
"s_cselect_b64 exec, 1, 0\n"
"buffer_load_dword v220, off, s[40:43], s69\n"
"buffer_load_dword v221, off, s[40:43], s70\n"
"buffer_load_dword v222, off, s[36:39], s23\n"
"s_mov_b64 exec, -1\n"
"s_waitcnt lgkmcnt(7)\n"
"v_mfma_f32_16x16x32_bf16 v[80:83], v[108:111], a[112:115], v[80:83]\n"
"v_lshlrev_b32 v212, 3, v9\n"
"v_mfma_f32_16x16x32_bf16 v[84:87], v[108:111], a[116:119], v[84:87]\n"
"v_lshl_add_u32 v212, v210, 1, v212\n"
"s_waitcnt lgkmcnt(6)\n"
"v_mfma_f32_16x16x32_bf16 v[88:91], v[112:115], a[112:115], v[88:91]\n"
"v_and_b32 v213, 1, v0\n"
"v_mfma_f32_16x16x32_bf16 v[92:95], v[112:115], a[116:119], v[92:95]\n"
"v_add_u32 v212, v212, v213\n"
"s_waitcnt lgkmcnt(0)\n"
"s_barrier\n"
"v_mfma_f32_16x16x32_bf16 v[56:59], v[116:119], a[120:123], v[56:59]\n"
"ds_read_b128 v[96:99], v1 offset:20480\n"
"v_lshlrev_b32 v212, 4, v212\n"
"v_mfma_f32_16x16x32_bf16 v[60:63], v[116:119], a[124:127], v[60:63]\n"
"ds_read_b128 v[100:103], v1 offset:21504\n"
"v_add_u32 v229, 104448, v212\n"
"v_mfma_f32_16x16x32_bf16 v[64:67], v[120:123], a[120:123], v[64:67]\n"
"ds_read_b128 v[104:107], v1 offset:22528\n"
"s_lshl_b32 s61, s59, 4\n"
"v_mfma_f32_16x16x32_bf16 v[68:71], v[120:123], a[124:127], v[68:71]\n"
"ds_read_b128 v[108:111], v1 offset:23552\n"
"v_add_u32 v216, s61, v8\n"
"v_mfma_f32_16x16x32_bf16 v[72:75], v[124:127], a[120:123], v[72:75]\n"
"ds_read_b128 v[112:115], v1 offset:24576\n"
"v_mul_u32_u24 v217, 80, v216\n"
"v_mfma_f32_16x16x32_bf16 v[76:79], v[124:127], a[124:127], v[76:79]\n"
"v_add_u32 v212, 8, v216\n"
"v_mfma_f32_16x16x32_bf16 v[80:83], v[128:131], a[120:123], v[80:83]\n"
"v_add_u32 v213, 24, v216\n"
"v_mfma_f32_16x16x32_bf16 v[84:87], v[128:131], a[124:127], v[84:87]\n"
"v_lshrrev_b32 v212, 5, v212\n"
"v_mfma_f32_16x16x32_bf16 v[88:91], v[132:135], a[120:123], v[88:91]\n"
"v_lshrrev_b32 v213, 5, v213\n"
"v_mfma_f32_16x16x32_bf16 v[92:95], v[132:135], a[124:127], v[92:95]\n"
"v_lshl_add_u32 v212, v212, 31, v217\n"
"s_waitcnt lgkmcnt(4)\n"
"v_mfma_f32_16x16x32_bf16 v[56:59], v[96:99], a[128:131], v[56:59]\n"
"ds_read_b128 v[116:119], v1 offset:25600\n"
"v_lshl_add_u32 v213, v213, 31, v217\n"
"v_mfma_f32_16x16x32_bf16 v[60:63], v[96:99], a[132:135], v[60:63]\n"
"ds_read_b128 v[120:123], v1 offset:26624\n"
"s_lshl_b32 s62, s49, 4\n"
"s_waitcnt lgkmcnt(5)\n"
"v_mfma_f32_16x16x32_bf16 v[64:67], v[100:103], a[128:131], v[64:67]\n"
"ds_read_b128 v[124:127], v1 offset:27648\n"
"v_lshl_add_u32 v216, v9, 2, s62\n"
"v_mfma_f32_16x16x32_bf16 v[68:71], v[100:103], a[132:135], v[68:71]\n"
"ds_read_b128 v[128:131], v1 offset:28672\n"
"v_lshlrev_b32 v217, 1, v216\n"
"s_waitcnt lgkmcnt(6)\n"
"v_mfma_f32_16x16x32_bf16 v[72:75], v[104:107], a[128:131], v[72:75]\n"
"ds_read_b128 v[132:135], v1 offset:29696\n"
"s_movk_i32 s63, -80\n"
"v_mfma_f32_16x16x32_bf16 v[76:79], v[104:107], a[132:135], v[76:79]\n"
"v_add_u32 v218, 24, v216\n"
"s_waitcnt lgkmcnt(6)\n"
"v_mfma_f32_16x16x32_bf16 v[80:83], v[108:111], a[128:131], v[80:83]\n"
"ds_read_b128 v[24:27], v232 offset:0\n"
"v_lshrrev_b32 v218, 6, v218\n"
"v_mfma_f32_16x16x32_bf16 v[84:87], v[108:111], a[132:135], v[84:87]\n"
"v_lshl_add_u32 v214, v218, 31, v217\n"
"s_waitcnt lgkmcnt(6)\n"
"v_mfma_f32_16x16x32_bf16 v[88:91], v[112:115], a[128:131], v[88:91]\n"
"ds_read_b128 v[28:31], v232 offset:64\n"
"v_mad_i32_i24 v214, v218, s63, v214\n"
"v_mfma_f32_16x16x32_bf16 v[92:95], v[112:115], a[132:135], v[92:95]\n"
"v_add_u32 v218, 40, v216\n"
"s_waitcnt lgkmcnt(6)\n"
"v_mfma_f32_16x16x32_bf16 v[56:59], v[116:119], a[136:139], v[56:59]\n"
"ds_read_b128 v[96:99], v1 offset:30720\n"
"v_lshrrev_b32 v218, 6, v218\n"
"v_mfma_f32_16x16x32_bf16 v[60:63], v[116:119], a[140:143], v[60:63]\n"
"ds_read_b128 v[100:103], v1 offset:31744\n"
"v_lshl_add_u32 v215, v218, 31, v217\n"
"s_waitcnt lgkmcnt(7)\n"
"v_mfma_f32_16x16x32_bf16 v[64:67], v[120:123], a[136:139], v[64:67]\n"
"ds_read_b128 v[104:107], v1 offset:32768\n"
"v_mad_i32_i24 v215, v218, s63, v215\n"
"v_mfma_f32_16x16x32_bf16 v[68:71], v[120:123], a[140:143], v[68:71]\n"
"ds_read_b128 v[108:111], v1 offset:33792\n"
"s_cmp_eq_u32 s49, 0\n"
"s_cselect_b32 s61, 0, 0x40000000\n"
"s_waitcnt lgkmcnt(8)\n"
"v_mfma_f32_16x16x32_bf16 v[72:75], v[124:127], a[136:139], v[72:75]\n"
"ds_read_b128 v[112:115], v1 offset:34816\n"
"s_and_b32 s62, s49, 1\n"
"v_mfma_f32_16x16x32_bf16 v[76:79], v[124:127], a[140:143], v[76:79]\n"
"s_lshl_b32 s62, s62, 30\n"
"s_waitcnt lgkmcnt(8)\n"
"v_mfma_f32_16x16x32_bf16 v[80:83], v[128:131], a[136:139], v[80:83]\n"
"ds_read_b128 v[32:35], v232 offset:128\n"
"s_add_u32 s62, s62, 1280\n"
"v_mfma_f32_16x16x32_bf16 v[84:87], v[128:131], a[140:143], v[84:87]\n"
"s_movk_i32 s63, 1312\n"
"s_waitcnt lgkmcnt(8)\n"
"v_mfma_f32_16x16x32_bf16 v[88:91], v[132:135], a[136:139], v[88:91]\n"
"ds_read_b128 v[36:39], v232 offset:192\n"
"v_add3_u32 v12, v212, v214, s61\n"
"v_mfma_f32_16x16x32_bf16 v[92:95], v[132:135], a[140:143], v[92:95]\n"
"v_add3_u32 v13, v213, v214, s62\n"
"s_waitcnt lgkmcnt(6)\n"
"v_mfma_f32_16x16x32_bf16 v[56:59], v[96:99], a[144:147], v[56:59]\n"
"ds_read_b128 v[116:119], v1 offset:35840\n"
"v_add3_u32 v14, v212, v215, 32\n"
"v_mfma_f32_16x16x32_bf16 v[60:63], v[96:99], a[148:151], v[60:63]\n"
"ds_read_b128 v[120:123], v1 offset:36864\n"
"v_add3_u32 v15, v213, v215, s63\n"
"s_waitcnt lgkmcnt(7)\n"
"v_mfma_f32_16x16x32_bf16 v[64:67], v[100:103], a[144:147], v[64:67]\n"
"ds_read_b128 v[124:127], v1 offset:37888\n"
"s_lshl_b32 s61, s53, 4\n"
"v_mfma_f32_16x16x32_bf16 v[68:71], v[100:103], a[148:151], v[68:71]\n"
"ds_read_b128 v[128:131], v1 offset:38912\n"
"s_add_u32 s62, s61, s52\n"
"s_waitcnt lgkmcnt(8)\n"
"v_mfma_f32_16x16x32_bf16 v[72:75], v[104:107], a[144:147], v[72:75]\n"
"ds_read_b128 v[132:135], v1 offset:39936\n"
"s_mul_i32 s16, s62, 3840\n"
"v_mfma_f32_16x16x32_bf16 v[76:79], v[104:107], a[148:151], v[76:79]\n"
"s_add_u32 s16, s16, 8372224\n"
"s_waitcnt lgkmcnt(8)\n"
"v_mfma_f32_16x16x32_bf16 v[80:83], v[108:111], a[144:147], v[80:83]\n"
"ds_read_b128 v[40:43], v232 offset:256\n"
"s_lshl_b32 s17, s53, 11\n"
"v_mfma_f32_16x16x32_bf16 v[84:87], v[108:111], a[148:151], v[84:87]\n"
"s_lshl_b32 s67, s52, 3\n"
"s_waitcnt lgkmcnt(8)\n"
"v_mfma_f32_16x16x32_bf16 v[88:91], v[112:115], a[144:147], v[88:91]\n"
"ds_read_b128 v[44:47], v232 offset:320\n"
"s_add_u32 s17, s17, s67\n"
"v_mfma_f32_16x16x32_bf16 v[92:95], v[112:115], a[148:151], v[92:95]\n"
"s_add_u32 s17, s17, 69632\n"
"s_waitcnt lgkmcnt(6)\n"
"v_mfma_f32_16x16x32_bf16 v[56:59], v[116:119], a[152:155], v[56:59]\n"
"v_mul_u32_u24 v212, 12, v210\n"
"v_mfma_f32_16x16x32_bf16 v[60:63], v[116:119], a[156:159], v[60:63]\n"
"v_add_u32 v212, v212, v211\n"
"s_waitcnt lgkmcnt(5)\n"
"v_mfma_f32_16x16x32_bf16 v[64:67], v[120:123], a[152:155], v[64:67]\n"
"v_mul_u32_u24 v212, 80, v212\n"
"v_mfma_f32_16x16x32_bf16 v[68:71], v[120:123], a[156:159], v[68:71]\n"
"v_add_u32 v212, 154112, v212\n"
"s_waitcnt lgkmcnt(4)\n"
"v_mfma_f32_16x16x32_bf16 v[72:75], v[124:127], a[152:155], v[72:75]\n"
"v_lshl_add_u32 v213, v209, 4, v212\n"
"v_mfma_f32_16x16x32_bf16 v[76:79], v[124:127], a[156:159], v[76:79]\n"
"v_lshl_add_u32 v214, v209, 3, v212\n"
"s_waitcnt lgkmcnt(3)\n"
"v_mfma_f32_16x16x32_bf16 v[80:83], v[128:131], a[152:155], v[80:83]\n"
"ds_read_b128 v[48:51], v232 offset:384\n"
"v_add_u32 v214, 64, v214\n"
"v_mfma_f32_16x16x32_bf16 v[84:87], v[128:131], a[156:159], v[84:87]\n"
"v_mov_b32 v215, 158208\n"
"s_waitcnt lgkmcnt(3)\n"
"v_mfma_f32_16x16x32_bf16 v[88:91], v[132:135], a[152:155], v[88:91]\n"
"ds_read_b128 v[52:55], v232 offset:448\n"
"s_mov_b32 s84, 0xff00ff\n"
"v_mfma_f32_16x16x32_bf16 v[92:95], v[132:135], a[156:159], v[92:95]\n"
"s_mov_b32 s85, 0xff00ff00\n"
"v_cndmask_b32 v233, v215, v213, s[84:85]\n"
"v_cndmask_b32 v234, v215, v214, s[84:85]\n"
"s_lshl_b32 s58, s49, 10\n"
"v_add_u32 v235, s58, v1\n"
"s_add_u32 s58, s58, 154112\n"
"v_lshl_add_u32 v212, v208, 4, v8\n"
"v_lshl_add_u32 v212, v212, 1, v209\n"
"v_lshl_add_u32 v236, v212, 3, s58\n"
"v_add_u32 v212, 256, v235\n"
"v_lshrrev_b32 v212, 12, v212\n"
"v_lshl_add_u32 v235, v212, 31, v235\n"
"s_mul_i32 s7, s53, 153600\n"
"s_lshl_b32 s13, s49, 12\n"
"s_add_u32 s7, s7, 3702784\n"
"s_add_u32 s7, s7, s13\n"
"s_mov_b32 s59, 36864\n"
"v_pk_fma_f32 v[56:57], v[24:25], v[16:17], v[56:57] op_sel:[0,0,0] op_sel_hi:[1,0,1]\n"
"v_pk_fma_f32 v[60:61], v[24:25], v[16:17], v[60:61] op_sel:[0,1,0] op_sel_hi:[1,1,1]\n"
"v_pk_fma_f32 v[58:59], v[26:27], v[16:17], v[58:59] op_sel:[0,0,0] op_sel_hi:[1,0,1]\n"
"v_pk_fma_f32 v[62:63], v[26:27], v[16:17], v[62:63] op_sel:[0,1,0] op_sel_hi:[1,1,1]\n"
"v_pk_fma_f32 v[64:65], v[28:29], v[16:17], v[64:65] op_sel:[0,0,0] op_sel_hi:[1,0,1]\n"
"v_pk_fma_f32 v[68:69], v[28:29], v[16:17], v[68:69] op_sel:[0,1,0] op_sel_hi:[1,1,1]\n"
"v_pk_fma_f32 v[66:67], v[30:31], v[16:17], v[66:67] op_sel:[0,0,0] op_sel_hi:[1,0,1]\n"
"v_pk_fma_f32 v[70:71], v[30:31], v[16:17], v[70:71] op_sel:[0,1,0] op_sel_hi:[1,1,1]\n"
"v_pk_fma_f32 v[72:73], v[32:33], v[16:17], v[72:73] op_sel:[0,0,0] op_sel_hi:[1,0,1]\n"
"v_pk_fma_f32 v[76:77], v[32:33], v[16:17], v[76:77] op_sel:[0,1,0] op_sel_hi:[1,1,1]\n"
"v_max3_f32 v136, v56, v57, v58\n"
"v_pk_fma_f32 v[80:81], v[36:37], v[16:17], v[80:81] op_sel:[0,0,0] op_sel_hi:[1,0,1]\n"
"v_max3_f32 v138, v60, v61, v62\n"
"v_pk_fma_f32 v[82:83], v[38:39], v[16:17], v[82:83] op_sel:[0,0,0] op_sel_hi:[1,0,1]\n"
"v_max3_f32 v137, -v56, -v57, -v58\n"
"s_waitcnt lgkmcnt(3)\n"
"v_pk_fma_f32 v[88:89], v[40:41], v[16:17], v[88:89] op_sel:[0,0,0] op_sel_hi:[1,0,1]\n"
"v_max3_f32 v139, -v60, -v61, -v62\n"
"v_pk_fma_f32 v[90:91], v[42:43], v[16:17], v[90:91] op_sel:[0,0,0] op_sel_hi:[1,0,1]\n"
"v_max3_f32 v136, v136, v59, v64\n"
"v_pk_fma_f32 v[74:75], v[34:35], v[16:17], v[74:75] op_sel:[0,0,0] op_sel_hi:[1,0,1]\n"
"v_max3_f32 v138, v138, v63, v68\n"
"v_pk_mul_f32 v[80:81], v[80:81], v[18:19] op_sel:[0,0] op_sel_hi:[1,0]\n"
"v_max3_f32 v137, v137, -v59, -v64\n"
"v_pk_mul_f32 v[82:83], v[82:83], v[18:19] op_sel:[0,0] op_sel_hi:[1,0]\n"
"v_max3_f32 v139, v139, -v63, -v68\n"
"v_pk_mul_f32 v[88:89], v[88:89], v[18:19] op_sel:[0,0] op_sel_hi:[1,0]\n"
"v_max3_f32 v136, v136, v65, v66\n"
"v_pk_mul_f32 v[90:91], v[90:91], v[18:19] op_sel:[0,0] op_sel_hi:[1,0]\n"
"v_max3_f32 v138, v138, v69, v70\n"
"v_pk_mul_f32 v[74:75], v[74:75], v[18:19] op_sel:[0,0] op_sel_hi:[1,0]\n"
"v_max3_f32 v137, v137, -v65, -v66\n"
"v_cvt_pk_bf16_f32 v160, v80, v81\n"
"v_max3_f32 v139, v139, -v69, -v70\n"
"v_cvt_pk_bf16_f32 v161, v82, v83\n"
"v_max3_f32 v136, v136, v67, v72\n"
"v_cvt_pk_bf16_f32 v162, v88, v89\n"
"v_max3_f32 v138, v138, v71, v76\n"
"v_cvt_pk_bf16_f32 v163, v90, v91\n"
"v_max3_f32 v137, v137, -v67, -v72\n"
"v_cvt_pk_bf16_f32 v164, v74, v75\n"
"v_max3_f32 v139, v139, -v71, -v76\n"
"v_mov_b32 v165, 0x3f80\n"
"v_max_f32 v136, v136, v73\n"
"ds_write_b64 v226, v[160:161] offset:0\n"
"v_max_f32 v138, v138, v77\n"
"ds_write_b64 v226, v[162:163] offset:8\n"
"v_max_f32 v137, v137, -v73\n"
"ds_write_b64 v226, v[164:165] offset:16\n"
"v_max_f32 v139, v139, -v77\n"
"v_pk_fma_f32 v[84:85], v[36:37], v[16:17], v[84:85] op_sel:[0,1,0] op_sel_hi:[1,1,1]\n"
"v_permlane16_swap_b32 v136, v137\n"
"v_pk_fma_f32 v[86:87], v[38:39], v[16:17], v[86:87] op_sel:[0,1,0] op_sel_hi:[1,1,1]\n"
"v_permlane16_swap_b32 v138, v139\n"
"v_pk_fma_f32 v[92:93], v[40:41], v[16:17], v[92:93] op_sel:[0,1,0] op_sel_hi:[1,1,1]\n"
"v_max_f32 v136, v136, v137\n"
"v_pk_fma_f32 v[94:95], v[42:43], v[16:17], v[94:95] op_sel:[0,1,0] op_sel_hi:[1,1,1]\n"
"v_max_f32 v138, v138, v139\n"
"v_pk_fma_f32 v[78:79], v[34:35], v[16:17], v[78:79] op_sel:[0,1,0] op_sel_hi:[1,1,1]\n"
"v_mov_b32 v137, v136\n"
"v_pk_mul_f32 v[84:85], v[84:85], v[18:19] op_sel:[0,1] op_sel_hi:[1,1]\n"
"buffer_load_dwordx4 v[176:179], v1, s[40:43], s59 offen sc1\n"
"buffer_load_dwordx4 v[180:183], v1, s[40:43], s59 offen offset:1024 sc1\n"
"v_mov_b32 v139, v138\n"
"v_pk_mul_f32 v[86:87], v[86:87], v[18:19] op_sel:[0,1] op_sel_hi:[1,1]\n"
"v_permlane16_swap_b32 v136, v137\n"
"v_pk_mul_f32 v[92:93], v[92:93], v[18:19] op_sel:[0,1] op_sel_hi:[1,1]\n"
"v_permlane16_swap_b32 v138, v139\n"
"v_pk_mul_f32 v[94:95], v[94:95], v[18:19] op_sel:[0,1] op_sel_hi:[1,1]\n"
"v_pk_mul_f32 v[136:137], v[136:137], v[20:21] op_sel:[0,0] op_sel_hi:[1,0] neg_lo:[1,0] neg_hi:[1,0]\n"
"v_pk_mul_f32 v[138:139], v[138:139], v[20:21] op_sel:[0,1] op_sel_hi:[1,1] neg_lo:[1,0] neg_hi:[1,0]\n"
"v_pk_fma_f32 v[96:97], v[56:57], v[20:21], v[136:137] op_sel:[0,0,0] op_sel_hi:[1,0,0]\n"
"v_pk_fma_f32 v[116:117], v[60:61], v[20:21], v[138:139] op_sel:[0,1,0] op_sel_hi:[1,1,0]\n"
"v_pk_fma_f32 v[106:107], v[56:57], v[20:21], v[136:137] op_sel:[0,0,1] op_sel_hi:[1,0,1] neg_lo:[1,0,0] neg_hi:[1,0,0]\n"
"v_pk_fma_f32 v[126:127], v[60:61], v[20:21], v[138:139] op_sel:[0,1,1] op_sel_hi:[1,1,1] neg_lo:[1,0,0] neg_hi:[1,0,0]\n"
"v_pk_fma_f32 v[98:99], v[58:59], v[20:21], v[136:137] op_sel:[0,0,0] op_sel_hi:[1,0,0]\n"
"v_pk_fma_f32 v[118:119], v[62:63], v[20:21], v[138:139] op_sel:[0,1,0] op_sel_hi:[1,1,0]\n"
"v_pk_fma_f32 v[108:109], v[58:59], v[20:21], v[136:137] op_sel:[0,0,1] op_sel_hi:[1,0,1] neg_lo:[1,0,0] neg_hi:[1,0,0]\n"
"v_pk_fma_f32 v[128:129], v[62:63], v[20:21], v[138:139] op_sel:[0,1,1] op_sel_hi:[1,1,1] neg_lo:[1,0,0] neg_hi:[1,0,0]\n"
"v_pk_fma_f32 v[100:101], v[64:65], v[20:21], v[136:137] op_sel:[0,0,0] op_sel_hi:[1,0,0]\n"
"v_pk_fma_f32 v[120:121], v[68:69], v[20:21], v[138:139] op_sel:[0,1,0] op_sel_hi:[1,1,0]\n"
"v_pk_fma_f32 v[110:111], v[64:65], v[20:21], v[136:137] op_sel:[0,0,1] op_sel_hi:[1,0,1] neg_lo:[1,0,0] neg_hi:[1,0,0]\n"
"v_pk_fma_f32 v[130:131], v[68:69], v[20:21], v[138:139] op_sel:[0,1,1] op_sel_hi:[1,1,1] neg_lo:[1,0,0] neg_hi:[1,0,0]\n"
"v_pk_fma_f32 v[102:103], v[66:67], v[20:21], v[136:137] op_sel:[0,0,0] op_sel_hi:[1,0,0]\n"
"v_pk_fma_f32 v[122:123], v[70:71], v[20:21], v[138:139] op_sel:[0,1,0] op_sel_hi:[1,1,0]\n"
"v_pk_fma_f32 v[112:113], v[66:67], v[20:21], v[136:137] op_sel:[0,0,1] op_sel_hi:[1,0,1] neg_lo:[1,0,0] neg_hi:[1,0,0]\n"
"v_pk_fma_f32 v[132:133], v[70:71], v[20:21], v[138:139] op_sel:[0,1,1] op_sel_hi:[1,1,1] neg_lo:[1,0,0] neg_hi:[1,0,0]\n"
"v_pk_fma_f32 v[104:105], v[72:73], v[20:21], v[136:137] op_sel:[0,0,0] op_sel_hi:[1,0,0]\n"
"v_pk_fma_f32 v[124:125], v[76:77], v[20:21], v[138:139] op_sel:[0,1,0] op_sel_hi:[1,1,0]\n"
"v_pk_fma_f32 v[114:115], v[72:73], v[20:21], v[136:137] op_sel:[0,0,1] op_sel_hi:[1,0,1] neg_lo:[1,0,0] neg_hi:[1,0,0]\n"
"v_pk_fma_f32 v[134:135], v[76:77], v[20:21], v[138:139] op_sel:[0,1,1] op_sel_hi:[1,1,1] neg_lo:[1,0,0] neg_hi:[1,0,0]\n"
"v_exp_f32 v96, v96\n"
"v_exp_f32 v116, v116\n"
"v_exp_f32 v106, v106\n"
"v_exp_f32 v126, v126\n"
"v_exp_f32 v97, v97\n"
"v_exp_f32 v117, v117\n"
"v_exp_f32 v107, v107\n"
"v_exp_f32 v127, v127\n"
"v_exp_f32 v98, v98\n"
"v_exp_f32 v118, v118\n"
"v_exp_f32 v108, v108\n"
"v_exp_f32 v128, v128\n"
"v_exp_f32 v99, v99\n"
"v_exp_f32 v119, v119\n"
"v_exp_f32 v109, v109\n"
"v_exp_f32 v129, v129\n"
"v_exp_f32 v100, v100\n"
"v_exp_f32 v120, v120\n"
"v_exp_f32 v110, v110\n"
"v_exp_f32 v130, v130\n"
"v_exp_f32 v101, v101\n"
"v_exp_f32 v121, v121\n"
"v_exp_f32 v111, v111\n"
"v_exp_f32 v131, v131\n"
"v_exp_f32 v102, v102\n"
"v_exp_f32 v122, v122\n"
"v_exp_f32 v112, v112\n"
"v_exp_f32 v132, v132\n"
"v_exp_f32 v103, v103\n"
"v_exp_f32 v123, v123\n"
"v_exp_f32 v113, v113\n"
"v_exp_f32 v133, v133\n"
"v_exp_f32 v104, v104\n"
"v_exp_f32 v124, v124\n"
"v_exp_f32 v114, v114\n"
"v_exp_f32 v134, v134\n"
"v_exp_f32 v105, v105\n"
"v_exp_f32 v125, v125\n"
"v_exp_f32 v115, v115\n"
"v_exp_f32 v135, v135\n"
"v_pk_add_f32 v[56:57], v[96:97], v[98:99]\n"
"v_pk_mul_f32 v[78:79], v[78:79], v[18:19] op_sel:[0,1] op_sel_hi:[1,1]\n"
"s_waitcnt vmcnt(0)\n"
"v_mov_b32 v183, v183\n"
"s_mov_b32 s67, 262144\n"
"v_cmp_ne_u64 vcc, s[56:57], v[176:177]\n"
"v_cmp_ne_u64 s[64:65], s[56:57], v[178:179]\n"
"s_or_b64 vcc, vcc, s[64:65]\n"
"v_cmp_ne_u64 s[64:65], s[56:57], v[180:181]\n"
"s_or_b64 vcc, vcc, s[64:65]\n"
"v_cmp_ne_u64 s[64:65], s[56:57], v[182:183]\n"
"s_or_b64 vcc, vcc, s[64:65]\n"
"s_cmp_eq_u64 vcc, 0\n"
"s_nop 1\n"
"s_cbranch_scc1 PD13_%=\n"
"PL12_%=:\n"
"buffer_load_dwordx4 v[176:179], v1, s[40:43], s59 offen sc1\n"
"buffer_load_dwordx4 v[180:183], v1, s[40:43], s59 offen offset:1024 sc1\n"
"s_waitcnt vmcnt(0)\n"
"v_cmp_ne_u64 vcc, s[56:57], v[176:177]\n"
"v_cmp_ne_u64 s[64:65], s[56:57], v[178:179]\n"
"s_or_b64 vcc, vcc, s[64:65]\n"
"v_cmp_ne_u64 s[64:65], s[56:57], v[180:181]\n"
"s_or_b64 vcc, vcc, s[64:65]\n"
"v_cmp_ne_u64 s[64:65], s[56:57], v[182:183]\n"
"s_or_b64 vcc, vcc, s[64:65]\n"
"s_cmp_eq_u64 vcc, 0\n"
"s_nop 1\n"
"s_cbranch_scc1 PD13_%=\n"
"s_sub_u32 s67, s67, 1\n"
"s_cbranch_scc0 PL12_%=\n"
"PD13_%=:\n"
"s_lshl_b32 s58, s48, 2\n"
"s_add_u32 s58, s58, s49\n"
"s_cmp_lg_u32 s58, 1\n"
"s_cbranch_scc1 EP14_%=\n"
"s_mov_b64 exec, 1\n"
"buffer_store_dwordx2 v[4:5], off, s[40:43], 0 sc1\n"
"s_mov_b64 exec, -1\n"
"EP14_%=:\n"
"buffer_load_dwordx4 a[160:163], v1, s[40:43], s6 offen offset:0 sc1\n"
"v_pk_add_f32 v[60:61], v[116:117], v[118:119]\n"
"v_cvt_pk_bf16_f32 v166, v84, v85\n"
"v_pk_add_f32 v[58:59], v[106:107], v[108:109]\n"
"v_cvt_pk_bf16_f32 v167, v86, v87\n"
"v_pk_add_f32 v[62:63], v[126:127], v[128:129]\n"
"v_cvt_pk_bf16_f32 v168, v92, v93\n"
"v_pk_add_f32 v[64:65], v[100:101], v[102:103]\n"
"v_cvt_pk_bf16_f32 v169, v94, v95\n"
"buffer_load_dwordx4 a[164:167], v1, s[40:43], s6 offen offset:1024 sc1\n"
"v_pk_add_f32 v[68:69], v[120:121], v[122:123]\n"
"v_cvt_pk_bf16_f32 v170, v78, v79\n"
"v_pk_add_f32 v[66:67], v[110:111], v[112:113]\n"
"v_mov_b32 v171, 0x3f80\n"
"v_pk_add_f32 v[70:71], v[130:131], v[132:133]\n"
"ds_write_b64 v226, v[166:167] offset:4608\n"
"v_pk_add_f32 v[56:57], v[56:57], v[104:105]\n"
"ds_write_b64 v226, v[168:169] offset:4616\n"
"buffer_load_dwordx4 a[168:171], v1, s[40:43], s6 offen offset:2048 sc1\n"
"v_pk_add_f32 v[60:61], v[60:61], v[124:125]\n"
"ds_write_b64 v226, v[170:171] offset:4624\n"
"v_pk_add_f32 v[58:59], v[58:59], v[114:115]\n"
"v_pk_add_f32 v[62:63], v[62:63], v[134:135]\n"
"v_pk_add_f32 v[56:57], v[56:57], v[64:65]\n"
"v_pk_add_f32 v[60:61], v[60:61], v[68:69]\n"
"v_pk_add_f32 v[58:59], v[58:59], v[66:67]\n"
"v_pk_add_f32 v[62:63], v[62:63], v[70:71]\n"
"v_add_f32 v136, v56, v57\n"
"buffer_load_dwordx4 a[172:175], v1, s[40:43], s6 offen offset:3072 sc1\n"
"v_add_f32 v138, v60, v61\n"
"v_add_f32 v137, v58, v59\n"
"v_add_f32 v139, v62, v63\n"
"s_nop 0\n"
"v_permlane16_swap_b32 v136, v137\n"
"v_permlane16_swap_b32 v138, v139\n"
"v_add_f32 v136, v136, v137\n"
"v_add_f32 v138, v138, v139\n"
"v_rcp_f32 v136, v136\n"
"s_add_u32 s6, s6, 4096\n"
"buffer_load_dwordx4 a[176:179], v1, s[40:43], s6 offen offset:0 sc1\n"
"v_rcp_f32 v138, v138\n"
"v_mov_b32 v137, v136\n"
"v_mov_b32 v139, v138\n"
"s_nop 0\n"
"v_permlane16_swap_b32 v136, v137\n"
"v_permlane16_swap_b32 v138, v139\n"
"v_pk_mul_f32 v[96:97], v[96:97], v[136:137] op_sel:[0,0] op_sel_hi:[1,0]\n"
"v_pk_mul_f32 v[116:117], v[116:117], v[138:139] op_sel:[0,0] op_sel_hi:[1,0]\n"
"v_pk_mul_f32 v[106:107], v[106:107], v[136:137] op_sel:[0,1] op_sel_hi:[1,1]\n"
"v_pk_mul_f32 v[126:127], v[126:127], v[138:139] op_sel:[0,1] op_sel_hi:[1,1]\n"
"buffer_load_dwordx4 a[180:183], v1, s[40:43], s6 offen offset:1024 sc1\n"
"v_pk_mul_f32 v[98:99], v[98:99], v[136:137] op_sel:[0,0] op_sel_hi:[1,0]\n"
"v_pk_mul_f32 v[118:119], v[118:119], v[138:139] op_sel:[0,0] op_sel_hi:[1,0]\n"
"v_pk_mul_f32 v[108:109], v[108:109], v[136:137] op_sel:[0,1] op_sel_hi:[1,1]\n"
"v_pk_mul_f32 v[128:129], v[128:129], v[138:139] op_sel:[0,1] op_sel_hi:[1,1]\n"
"v_pk_mul_f32 v[100:101], v[100:101], v[136:137] op_sel:[0,0] op_sel_hi:[1,0]\n"
"v_pk_mul_f32 v[120:121], v[120:121], v[138:139] op_sel:[0,0] op_sel_hi:[1,0]\n"
"v_pk_mul_f32 v[110:111], v[110:111], v[136:137] op_sel:[0,1] op_sel_hi:[1,1]\n"
"v_pk_mul_f32 v[130:131], v[130:131], v[138:139] op_sel:[0,1] op_sel_hi:[1,1]\n"
"buffer_load_dwordx4 a[184:187], v1, s[40:43], s6 offen offset:2048 sc1\n"
"v_pk_mul_f32 v[102:103], v[102:103], v[136:137] op_sel:[0,0] op_sel_hi:[1,0]\n"
"v_pk_mul_f32 v[122:123], v[122:123], v[138:139] op_sel:[0,0] op_sel_hi:[1,0]\n"
"v_pk_mul_f32 v[112:113], v[112:113], v[136:137] op_sel:[0,1] op_sel_hi:[1,1]\n"
"v_pk_mul_f32 v[132:133], v[132:133], v[138:139] op_sel:[0,1] op_sel_hi:[1,1]\n"
"v_pk_mul_f32 v[104:105], v[104:105], v[136:137] op_sel:[0,0] op_sel_hi:[1,0]\n"
"v_pk_mul_f32 v[124:125], v[124:125], v[138:139] op_sel:[0,0] op_sel_hi:[1,0]\n"
"v_pk_mul_f32 v[114:115], v[114:115], v[136:137] op_sel:[0,1] op_sel_hi:[1,1]\n"
"v_pk_mul_f32 v[134:135], v[134:135], v[138:139] op_sel:[0,1] op_sel_hi:[1,1]\n"
"buffer_load_dwordx4 a[188:191], v1, s[40:43], s6 offen offset:3072 sc1\n"
"v_cvt_pk_bf16_f32 v140, v96, v97\n"
"v_cvt_pk_bf16_f32 v150, v116, v117\n"
"v_cvt_pk_bf16_f32 v144, v106, v107\n"
"v_cvt_pk_bf16_f32 v154, v126, v127\n"
"v_cvt_pk_bf16_f32 v141, v98, v99\n"
"v_cvt_pk_bf16_f32 v151, v118, v119\n"
"v_cvt_pk_bf16_f32 v145, v108, v109\n"
"v_cvt_pk_bf16_f32 v155, v128, v129\n"
"v_cvt_pk_bf16_f32 v142, v100, v101\n"
"s_add_u32 s6, s6, 4096\n"
"buffer_load_dwordx4 a[192:195], v1, s[40:43], s6 offen offset:0 sc1\n"
"v_cvt_pk_bf16_f32 v152, v120, v121\n"
"v_cvt_pk_bf16_f32 v146, v110, v111\n"
"v_cvt_pk_bf16_f32 v156, v130, v131\n"
"v_cvt_pk_bf16_f32 v143, v102, v103\n"
"v_cvt_pk_bf16_f32 v153, v122, v123\n"
"v_cvt_pk_bf16_f32 v147, v112, v113\n"
"v_cvt_pk_bf16_f32 v157, v132, v133\n"
"v_cvt_pk_bf16_f32 v148, v104, v105\n"
"buffer_load_dwordx4 a[196:199], v1, s[40:43], s6 offen offset:1024 sc1\n"
"v_cvt_pk_bf16_f32 v158, v124, v125\n"
"v_cvt_pk_bf16_f32 v149, v114, v115\n"
"v_cvt_pk_bf16_f32 v159, v134, v135\n"
"ds_write_b128 v224, v[140:143] offset:0\n"
"ds_write_b128 v224, v[150:153] offset:4608\n"
"ds_write_b128 v224, v[144:147] offset:32\n"
"ds_write_b128 v224, v[154:157] offset:4640\n"
"ds_write_b64 v225, v[148:149] offset:0\n"
"ds_write_b64 v225, v[158:159] offset:4608\n"
"s_waitcnt lgkmcnt(0)\n"
"s_barrier\n"
"ds_read_b64_tr_b16 v[96:97], v227 offset:0\n"
"ds_read_b64_tr_b16 v[104:105], v228 offset:0\n"
"ds_read_b64_tr_b16 v[98:99], v227 offset:4608\n"
"ds_read_b64_tr_b16 v[106:107], v228 offset:4608\n"
"ds_read_b64_tr_b16 v[100:101], v227 offset:32\n"
"ds_read_b64_tr_b16 v[108:109], v228 offset:32\n"
"ds_read_b64_tr_b16 v[102:103], v227 offset:4640\n"
"ds_read_b64_tr_b16 v[110:111], v228 offset:4640\n"
"buffer_load_dwordx4 a[200:203], v1, s[40:43], s6 offen offset:2048 sc1\n"
"buffer_load_dwordx4 a[204:207], v1, s[40:43], s6 offen offset:3072 sc1\n"
"ds_read_b64_tr_b16 v[112:113], v227 offset:9216\n"
"ds_read_b64_tr_b16 v[120:121], v228 offset:9216\n"
"ds_read_b64_tr_b16 v[114:115], v227 offset:13824\n"
"ds_read_b64_tr_b16 v[122:123], v228 offset:13824\n"
"ds_read_b64_tr_b16 v[116:117], v227 offset:9248\n"
"ds_read_b64_tr_b16 v[124:125], v228 offset:9248\n"
"ds_read_b64_tr_b16 v[118:119], v227 offset:13856\n"
"ds_read_b64_tr_b16 v[126:127], v228 offset:13856\n"
"s_waitcnt lgkmcnt(12)\n"
"v_mfma_f32_16x16x32_bf16 v[128:131], v[96:99], v[104:107], 0\n"
"s_waitcnt lgkmcnt(8)\n"
"v_mfma_f32_16x16x32_bf16 v[132:135], v[96:99], v[108:111], 0\n"
"v_mfma_f32_16x16x32_bf16 v[136:139], v[100:103], v[104:107], 0\n"
"v_mfma_f32_16x16x32_bf16 v[140:143], v[100:103], v[108:111], 0\n"
"ds_read_b64_tr_b16 v[96:97], v227 offset:18432\n"
"ds_read_b64_tr_b16 v[104:105], v228 offset:18432\n"
"ds_read_b64_tr_b16 v[98:99], v227 offset:23040\n"
"ds_read_b64_tr_b16 v[106:107], v228 offset:23040\n"
"ds_read_b64_tr_b16 v[100:101], v227 offset:18464\n"
"ds_read_b64_tr_b16 v[108:109], v228 offset:18464\n"
"ds_read_b64_tr_b16 v[102:103], v227 offset:23072\n"
"ds_read_b64_tr_b16 v[110:111], v228 offset:23072\n"
"s_waitcnt lgkmcnt(12)\n"
"v_mfma_f32_16x16x32_bf16 v[128:131], v[112:115], v[120:123], v[128:131]\n"
"s_waitcnt lgkmcnt(8)\n"
"v_mfma_f32_16x16x32_bf16 v[132:135], v[112:115], v[124:127], v[132:135]\n"
"v_mfma_f32_16x16x32_bf16 v[136:139], v[116:119], v[120:123], v[136:139]\n"
"v_mfma_f32_16x16x32_bf16 v[140:143], v[116:119], v[124:127], v[140:143]\n"
"ds_read_b64_tr_b16 v[112:113], v227 offset:27648\n"
"ds_read_b64_tr_b16 v[120:121], v228 offset:27648\n"
"ds_read_b64_tr_b16 v[114:115], v227 offset:32256\n"
"ds_read_b64_tr_b16 v[122:123], v228 offset:32256\n"
"ds_read_b64_tr_b16 v[116:117], v227 offset:27680\n"
"PAD3_%=:\n"
"s_cbranch_execz PADRET_%=\n"
"ds_read_b64_tr_b16 v[124:125], v228 offset:27680\n"
"ds_read_b64_tr_b16 v[118:119], v227 offset:32288\n"
"ds_read_b64_tr_b16 v[126:127], v228 offset:32288\n"
"s_waitcnt lgkmcnt(12)\n"
"v_mfma_f32_16x16x32_bf16 v[128:131], v[96:99], v[104:107], v[128:131]\n"
"s_waitcnt lgkmcnt(8)\n"
"v_mfma_f32_16x16x32_bf16 v[132:135], v[96:99], v[108:111], v[132:135]\n"
"v_mfma_f32_16x16x32_bf16 v[136:139], v[100:103], v[104:107], v[136:139]\n"
"v_mfma_f32_16x16x32_bf16 v[140:143], v[100:103], v[108:111], v[140:143]\n"
"s_waitcnt lgkmcnt(4)\n"
"v_mfma_f32_16x16x32_bf16 v[128:131], v[112:115], v[120:123], v[128:131]\n"
"s_waitcnt lgkmcnt(0)\n"
"v_mfma_f32_16x16x32_bf16 v[132:135], v[112:115], v[124:127], v[132:135]\n"
"v_mfma_f32_16x16x32_bf16 v[136:139], v[116:119], v[120:123], v[136:139]\n"
"v_mfma_f32_16x16x32_bf16 v[140:143], v[116:119], v[124:127], v[140:143]\n"
"s_barrier\n"
"ds_read_b128 v[96:99], v2 offset:36864\n"
"ds_read_b128 v[100:103], v2 offset:37888\n"
"ds_read_b128 v[104:107], v229 offset:0\n"
"v_cvt_pk_bf16_f32 v128, v128, v129\n"
"v_cvt_pk_bf16_f32 v129, v130, v131\n"
"buffer_store_dwordx2 v[128:129], v12, s[40:43], s16 offen sc1\n"
"v_cvt_pk_bf16_f32 v132, v132, v133\n"
"v_cvt_pk_bf16_f32 v133, v134, v135\n"
"buffer_store_dwordx2 v[132:133], v13, s[40:43], s16 offen sc1\n"
"v_cvt_pk_bf16_f32 v136, v136, v137\n"
"v_cvt_pk_bf16_f32 v137, v138, v139\n"
"buffer_store_dwordx2 v[136:137], v14, s[40:43], s16 offen sc1\n"
"v_cvt_pk_bf16_f32 v140, v140, v141\n"
"v_cvt_pk_bf16_f32 v141, v142, v143\n"
"buffer_store_dwordx2 v[140:141], v15, s[40:43], s16 offen sc1\n"
"s_mul_i32 s59, s53, 153600\n"
"s_add_u32 s59, s59, 3846144\n"
"s_add_u32 s60, s59, 4096\n"
"s_waitcnt lgkmcnt(2)\n"
"v_mfma_f32_16x16x32_bf16 v[56:59], v[96:99], a[0:3], 0\n"
"ds_read_b128 v[108:111], v2 offset:39424\n"
"v_mfma_f32_16x16x32_bf16 v[60:63], v[96:99], a[4:7], 0\n"
"ds_read_b128 v[112:115], v2 offset:40448\n"
"s_waitcnt lgkmcnt(3)\n"
"v_mfma_f32_16x16x32_bf16 v[64:67], v[100:103], a[0:3], 0\n"
"ds_read_b128 v[116:119], v229 offset:2560\n"
"v_mfma_f32_16x16x32_bf16 v[68:71], v[100:103], a[4:7], 0\n"
"s_add_u32 s6, s6, 4096\n"
"buffer_load_dwordx4 a[208:211], v1, s[40:43], s6 offen offset:0 sc1\n"
"s_waitcnt lgkmcnt(3)\n"
"v_mfma_f32_16x16x32_bf16 v[72:75], v[104:107], a[0:3], 0\n"
"buffer_load_dword v176, v230, s[40:43], s59 offen offset:0 sc1\n"
"v_mfma_f32_16x16x32_bf16 v[76:79], v[104:107], a[4:7], 0\n"
"buffer_load_dwordx4 a[212:215], v1, s[40:43], s6 offen offset:1024 sc1\n"
"s_waitcnt lgkmcnt(2)\n"
"v_mfma_f32_16x16x32_bf16 v[56:59], v[108:111], a[8:11], v[56:59]\n"
"ds_read_b128 v[96:99], v2 offset:41984\n"
"v_and_b32 v88, 1, v0\n"
"v_mfma_f32_16x16x32_bf16 v[60:63], v[108:111], a[12:15], v[60:63]\n"
"ds_read_b128 v[100:103], v2 offset:43008\n"
"v_lshlrev_b32 v88, 4, v88\n"
"s_waitcnt lgkmcnt(3)\n"
"v_mfma_f32_16x16x32_bf16 v[64:67], v[112:115], a[8:11], v[64:67]\n"
"ds_read_b128 v[104:107], v229 offset:5120\n"
"v_mov_b32 v89, 0x3f80\n"
"v_mfma_f32_16x16x32_bf16 v[68:71], v[112:115], a[12:15], v[68:71]\n"
"s_waitcnt lgkmcnt(3)\n"
"v_mfma_f32_16x16x32_bf16 v[72:75], v[116:119], a[8:11], v[72:75]\n"
"buffer_load_dword v177, v230, s[40:43], s59 offen offset:512 sc1\n"
"v_mfma_f32_16x16x32_bf16 v[76:79], v[116:119], a[12:15], v[76:79]\n"
"buffer_load_dwordx4 a[216:219], v1, s[40:43], s6 offen offset:2048 sc1\n"
"s_waitcnt lgkmcnt(2)\n"
"v_mfma_f32_16x16x32_bf16 v[56:59], v[96:99], a[16:19], v[56:59]\n"
"ds_read_b128 v[108:111], v2 offset:44544\n"
"v_lshlrev_b32 v88, v88, v89\n"
"v_mfma_f32_16x16x32_bf16 v[60:63], v[96:99], a[20:23], v[60:63]\n"
"ds_read_b128 v[112:115], v2 offset:45568\n"
"v_lshrrev_b32 v89, 3, v0\n"
"s_waitcnt lgkmcnt(3)\n"
"v_mfma_f32_16x16x32_bf16 v[64:67], v[100:103], a[16:19], v[64:67]\n"
"ds_read_b128 v[116:119], v229 offset:7680\n"
"v_lshrrev_b32 v90, 4, v0\n"
"v_mfma_f32_16x16x32_bf16 v[68:71], v[100:103], a[20:23], v[68:71]\n"
"s_waitcnt lgkmcnt(3)\n"
"v_mfma_f32_16x16x32_bf16 v[72:75], v[104:107], a[16:19], v[72:75]\n"
"buffer_load_dword v178, v230, s[40:43], s59 offen offset:1024 sc1\n"
"v_mfma_f32_16x16x32_bf16 v[76:79], v[104:107], a[20:23], v[76:79]\n"
"buffer_load_dwordx4 a[220:223], v1, s[40:43], s6 offen offset:3072 sc1\n"
"s_waitcnt lgkmcnt(2)\n"
"v_mfma_f32_16x16x32_bf16 v[56:59], v[108:111], a[24:27], v[56:59]\n"
"ds_read_b128 v[96:99], v2 offset:47104\n"
"v_xor_b32 v89, v89, v90\n"
"v_mfma_f32_16x16x32_bf16 v[60:63], v[108:111], a[28:31], v[60:63]\n"
"ds_read_b128 v[100:103], v2 offset:48128\n"
"v_and_b32 v89, 1, v89\n"
"s_waitcnt lgkmcnt(3)\n"
"v_mfma_f32_16x16x32_bf16 v[64:67], v[112:115], a[24:27], v[64:67]\n"
"ds_read_b128 v[104:107], v229 offset:10240\n"
"v_lshrrev_b32 v90, 5, v0\n"
"v_mfma_f32_16x16x32_bf16 v[68:71], v[112:115], a[28:31], v[68:71]\n"
"s_add_u32 s6, s6, 4096\n"
"buffer_load_dwordx4 a[224:227], v1, s[40:43], s6 offen offset:0 sc1\n"
"s_waitcnt lgkmcnt(3)\n"
"v_mfma_f32_16x16x32_bf16 v[72:75], v[116:119], a[24:27], v[72:75]\n"
"buffer_load_dword v179, v230, s[40:43], s59 offen offset:1536 sc1\n"
"v_mfma_f32_16x16x32_bf16 v[76:79], v[116:119], a[28:31], v[76:79]\n"
"s_waitcnt lgkmcnt(2)\n"
"v_mfma_f32_16x16x32_bf16 v[56:59], v[96:99], a[32:35], v[56:59]\n"
"ds_read_b128 v[108:111], v2 offset:49664\n"
"v_lshl_add_u32 v90, v89, 1, v90\n"
"v_mfma_f32_16x16x32_bf16 v[60:63], v[96:99], a[36:39], v[60:63]\n"
"ds_read_b128 v[112:115], v2 offset:50688\n"
"v_bfe_u32 v89, v0, 1, 2\n"
"s_waitcnt lgkmcnt(3)\n"
"v_mfma_f32_16x16x32_bf16 v[64:67], v[100:103], a[32:35], v[64:67]\n"
"ds_read_b128 v[116:119], v229 offset:12800\n"
"v_lshl_add_u32 v90, v90, 2, v89\n"
"v_mfma_f32_16x16x32_bf16 v[68:71], v[100:103], a[36:39], v[68:71]\n"
"buffer_load_dwordx4 a[228:231], v1, s[40:43], s6 offen offset:1024 sc1\n"
"s_waitcnt lgkmcnt(3)\n"
"v_mfma_f32_16x16x32_bf16 v[72:75], v[104:107], a[32:35], v[72:75]\n"
"buffer_load_dword v180, v230, s[40:43], s59 offen offset:2048 sc1\n"
"v_mfma_f32_16x16x32_bf16 v[76:79], v[104:107], a[36:39], v[76:79]\n"
"s_waitcnt lgkmcnt(2)\n"
"v_mfma_f32_16x16x32_bf16 v[56:59], v[108:111], a[40:43], v[56:59]\n"
"ds_read_b128 v[96:99], v2 offset:52224\n"
"v_lshlrev_b32 v90, v90, 1\n"
"v_mfma_f32_16x16x32_bf16 v[60:63], v[108:111], a[44:47], v[60:63]\n"
"ds_read_b128 v[100:103], v2 offset:53248\n"
"v_bfe_i32 v80, v90, 0, 1\n"
"s_waitcnt lgkmcnt(3)\n"
"v_mfma_f32_16x16x32_bf16 v[64:67], v[112:115], a[40:43], v[64:67]\n"
"ds_read_b128 v[104:107], v229 offset:15360\n"
"v_and_b32 v80, v80, v88\n"
"v_mfma_f32_16x16x32_bf16 v[68:71], v[112:115], a[44:47], v[68:71]\n"
"buffer_load_dwordx4 a[232:235], v1, s[40:43], s6 offen offset:2048 sc1\n"
"s_waitcnt lgkmcnt(3)\n"
"v_mfma_f32_16x16x32_bf16 v[72:75], v[116:119], a[40:43], v[72:75]\n"
"buffer_load_dword v181, v230, s[40:43], s59 offen offset:2560 sc1\n"
"v_mfma_f32_16x16x32_bf16 v[76:79], v[116:119], a[44:47], v[76:79]\n"
"s_waitcnt lgkmcnt(2)\n"
"v_mfma_f32_16x16x32_bf16 v[56:59], v[96:99], a[48:51], v[56:59]\n"
"ds_read_b128 v[108:111], v2 offset:54784\n"
"v_bfe_i32 v81, v90, 1, 1\n"
"v_mfma_f32_16x16x32_bf16 v[60:63], v[96:99], a[52:55], v[60:63]\n"
"ds_read_b128 v[112:115], v2 offset:55808\n"
"v_and_b32 v81, v81, v88\n"
"s_waitcnt lgkmcnt(3)\n"
"v_mfma_f32_16x16x32_bf16 v[64:67], v[100:103], a[48:51], v[64:67]\n"
"ds_read_b128 v[116:119], v229 offset:17920\n"
"v_bfe_i32 v82, v90, 2, 1\n"
"v_mfma_f32_16x16x32_bf16 v[68:71], v[100:103], a[52:55], v[68:71]\n"
"buffer_load_dwordx4 a[236:239], v1, s[40:43], s6 offen offset:3072 sc1\n"
"s_waitcnt lgkmcnt(3)\n"
"v_mfma_f32_16x16x32_bf16 v[72:75], v[104:107], a[48:51], v[72:75]\n"
"buffer_load_dword v182, v230, s[40:43], s59 offen offset:3072 sc1\n"
"v_mfma_f32_16x16x32_bf16 v[76:79], v[104:107], a[52:55], v[76:79]\n"
"s_add_u32 s6, s6, 4096\n"
"buffer_load_dwordx4 a[240:243], v1, s[40:43], s6 offen offset:0 sc1\n"
"s_waitcnt lgkmcnt(2)\n"
"v_mfma_f32_16x16x32_bf16 v[56:59], v[108:111], a[56:59], v[56:59]\n"
"ds_read_b128 v[96:99], v2 offset:57344\n"
"v_and_b32 v82, v82, v88\n"
"v_mfma_f32_16x16x32_bf16 v[60:63], v[108:111], a[60:63], v[60:63]\n"
"ds_read_b128 v[100:103], v2 offset:58368\n"
"v_bfe_i32 v83, v90, 3, 1\n"
"s_waitcnt lgkmcnt(3)\n"
"v_mfma_f32_16x16x32_bf16 v[64:67], v[112:115], a[56:59], v[64:67]\n"
"ds_read_b128 v[104:107], v229 offset:20480\n"
"v_and_b32 v83, v83, v88\n"
"v_mfma_f32_16x16x32_bf16 v[68:71], v[112:115], a[60:63], v[68:71]\n"
"s_waitcnt lgkmcnt(3)\n"
"v_mfma_f32_16x16x32_bf16 v[72:75], v[116:119], a[56:59], v[72:75]\n"
"buffer_load_dword v183, v230, s[40:43], s59 offen offset:3584 sc1\n"
"v_mfma_f32_16x16x32_bf16 v[76:79], v[116:119], a[60:63], v[76:79]\n"
"buffer_load_dwordx4 a[244:247], v1, s[40:43], s6 offen offset:1024 sc1\n"
"s_waitcnt vmcnt(18)\n"
"s_barrier\n"
"s_cmp_lg_u32 s49, 0\n"
"s_nop 2\n"
"s_cbranch_scc1 FS15_%=\n"
"s_mov_b64 exec, 1\n"
"buffer_store_dwordx2 v[4:5], off, s[40:43], s17 sc1\n"
"s_mov_b64 exec, -1\n"
"FS15_%=:\n"
"s_waitcnt lgkmcnt(2)\n"
"v_mfma_f32_16x16x32_bf16 v[56:59], v[96:99], a[64:67], v[56:59]\n"
"ds_read_b128 v[108:111], v2 offset:59904\n"
"v_bfe_i32 v84, v90, 4, 1\n"
"v_mfma_f32_16x16x32_bf16 v[60:63], v[96:99], a[68:71], v[60:63]\n"
"ds_read_b128 v[112:115], v2 offset:60928\n"
"v_and_b32 v84, v84, v88\n"
"s_waitcnt lgkmcnt(3)\n"
"v_mfma_f32_16x16x32_bf16 v[64:67], v[100:103], a[64:67], v[64:67]\n"
"ds_read_b128 v[116:119], v229 offset:23040\n"
"v_bfe_i32 v85, v90, 5, 1\n"
"v_mfma_f32_16x16x32_bf16 v[68:71], v[100:103], a[68:71], v[68:71]\n"
"s_waitcnt lgkmcnt(3)\n"
"v_mfma_f32_16x16x32_bf16 v[72:75], v[104:107], a[64:67], v[72:75]\n"
"buffer_load_dword v184, v230, s[40:43], s60 offen offset:0 sc1\n"
"v_mfma_f32_16x16x32_bf16 v[76:79], v[104:107], a[68:71], v[76:79]\n"
"buffer_load_dwordx4 a[248:251], v1, s[40:43], s6 offen offset:2048 sc1\n"
"s_waitcnt lgkmcnt(2)\n"
"v_mfma_f32_16x16x32_bf16 v[56:59], v[108:111], a[72:75], v[56:59]\n"
"ds_read_b128 v[96:99], v2 offset:62464\n"
"v_and_b32 v85, v85, v88\n"
"v_mfma_f32_16x16x32_bf16 v[60:63], v[108:111], a[76:79], v[60:63]\n"
"ds_read_b128 v[100:103], v2 offset:63488\n"
"v_bfe_i32 v86, v90, 6, 1\n"
"s_waitcnt lgkmcnt(3)\n"
"v_mfma_f32_16x16x32_bf16 v[64:67], v[112:115], a[72:75], v[64:67]\n"
"ds_read_b128 v[104:107], v229 offset:25600\n"
"v_and_b32 v86, v86, v88\n"
"v_mfma_f32_16x16x32_bf16 v[68:71], v[112:115], a[76:79], v[68:71]\n"
"s_waitcnt lgkmcnt(3)\n"
"v_mfma_f32_16x16x32_bf16 v[72:75], v[116:119], a[72:75], v[72:75]\n"
"buffer_load_dword v185, v230, s[40:43], s60 offen offset:512 sc1\n"
"v_mfma_f32_16x16x32_bf16 v[76:79], v[116:119], a[76:79], v[76:79]\n"
"buffer_load_dwordx4 a[252:255], v1, s[40:43], s6 offen offset:3072 sc1\n"
"s_waitcnt lgkmcnt(2)\n"
"v_mfma_f32_16x16x32_bf16 v[56:59], v[96:99], a[80:83], v[56:59]\n"
"ds_read_b128 v[108:111], v2 offset:65024\n"
"v_bfe_i32 v87, v90, 7, 1\n"
"v_mfma_f32_16x16x32_bf16 v[60:63], v[96:99], a[84:87], v[60:63]\n"
"ds_read_b128 v[112:115], v3 offset:512\n"
"v_and_b32 v87, v87, v88\n"
"s_waitcnt lgkmcnt(3)\n"
"v_mfma_f32_16x16x32_bf16 v[64:67], v[100:103], a[80:83], v[64:67]\n"
"ds_read_b128 v[116:119], v229 offset:28160\n"
"v_mfma_f32_16x16x32_bf16 v[68:71], v[100:103], a[84:87], v[68:71]\n"
"s_add_u32 s6, s6, 4096\n"
"buffer_load_dwordx4 v[252:255], v1, s[40:43], s6 offen offset:0 sc1\n"
"s_waitcnt lgkmcnt(3)\n"
"v_mfma_f32_16x16x32_bf16 v[72:75], v[104:107], a[80:83], v[72:75]\n"
"v_mfma_f32_16x16x32_bf16 v[76:79], v[104:107], a[84:87], v[76:79]\n"
"s_waitcnt lgkmcnt(2)\n"
"v_mfma_f32_16x16x32_bf16 v[56:59], v[108:111], a[88:91], v[56:59]\n"
"ds_read_b128 v[96:99], v3 offset:2048\n"
"v_mfma_f32_16x16x32_bf16 v[60:63], v[108:111], a[92:95], v[60:63]\n"
"ds_read_b128 v[100:103], v3 offset:3072\n"
"s_waitcnt lgkmcnt(3)\n"
"v_mfma_f32_16x16x32_bf16 v[64:67], v[112:115], a[88:91], v[64:67]\n"
"ds_read_b128 v[104:107], v229 offset:30720\n"
"v_mfma_f32_16x16x32_bf16 v[68:71], v[112:115], a[92:95], v[68:71]\n"
"s_add_u32 m0, s13, 0\n"
"s_add_u32 s18, s7, 0\n"
"buffer_load_dwordx4 v1, s[40:43], s18 offen offset:0 sc1 lds\n"
"s_waitcnt lgkmcnt(3)\n"
"v_mfma_f32_16x16x32_bf16 v[72:75], v[116:119], a[88:91], v[72:75]\n"
"v_mfma_f32_16x16x32_bf16 v[76:79], v[116:119], a[92:95], v[76:79]\n"
"s_waitcnt lgkmcnt(2)\n"
"v_mfma_f32_16x16x32_bf16 v[56:59], v[96:99], a[96:99], v[56:59]\n"
"ds_read_b128 v[108:111], v3 offset:4608\n"
"v_mfma_f32_16x16x32_bf16 v[60:63], v[96:99], a[100:103], v[60:63]\n"
"ds_read_b128 v[112:115], v3 offset:5632\n"
"s_waitcnt lgkmcnt(3)\n"
"v_mfma_f32_16x16x32_bf16 v[64:67], v[100:103], a[96:99], v[64:67]\n"
"ds_read_b128 v[116:119], v229 offset:33280\n"
"v_mfma_f32_16x16x32_bf16 v[68:71], v[100:103], a[100:103], v[68:71]\n"
"buffer_load_dwordx4 v1, s[40:43], s18 offen offset:1024 sc1 lds\n"
"s_waitcnt lgkmcnt(3)\n"
"v_mfma_f32_16x16x32_bf16 v[72:75], v[104:107], a[96:99], v[72:75]\n"
"v_mfma_f32_16x16x32_bf16 v[76:79], v[104:107], a[100:103], v[76:79]\n"
"buffer_load_dwordx4 v1, s[40:43], s18 offen offset:2048 sc1 lds\n"
"s_waitcnt lgkmcnt(2)\n"
"v_mfma_f32_16x16x32_bf16 v[56:59], v[108:111], a[104:107], v[56:59]\n"
"ds_read_b128 v[96:99], v3 offset:7168\n"
"v_mfma_f32_16x16x32_bf16 v[60:63], v[108:111], a[108:111], v[60:63]\n"
"ds_read_b128 v[100:103], v3 offset:8192\n"
"s_waitcnt lgkmcnt(3)\n"
"v_mfma_f32_16x16x32_bf16 v[64:67], v[112:115], a[104:107], v[64:67]\n"
"ds_read_b128 v[104:107], v229 offset:35840\n"
"v_mfma_f32_16x16x32_bf16 v[68:71], v[112:115], a[108:111], v[68:71]\n"
"s_waitcnt lgkmcnt(3)\n"
"v_mfma_f32_16x16x32_bf16 v[72:75], v[116:119], a[104:107], v[72:75]\n"
"v_mfma_f32_16x16x32_bf16 v[76:79], v[116:119], a[108:111], v[76:79]\n"
"buffer_load_dwordx4 v1, s[40:43], s18 offen offset:3072 sc1 lds\n"
"s_waitcnt lgkmcnt(2)\n"
"v_mfma_f32_16x16x32_bf16 v[56:59], v[96:99], a[112:115], v[56:59]\n"
"ds_read_b128 v[108:111], v3 offset:9728\n"
"v_mfma_f32_16x16x32_bf16 v[60:63], v[96:99], a[116:119], v[60:63]\n"
"ds_read_b128 v[112:115], v3 offset:10752\n"
"s_waitcnt lgkmcnt(3)\n"
"v_mfma_f32_16x16x32_bf16 v[64:67], v[100:103], a[112:115], v[64:67]\n"
"ds_read_b128 v[116:119], v229 offset:38400\n"
"v_mfma_f32_16x16x32_bf16 v[68:71], v[100:103], a[116:119], v[68:71]\n"
"s_waitcnt lgkmcnt(3)\n"
"v_mfma_f32_16x16x32_bf16 v[72:75], v[104:107], a[112:115], v[72:75]\n"
"s_waitcnt vmcnt(23)\n"
"v_add_f32 v176, v176, v177\n"
"s_waitcnt vmcnt(21)\n"
"v_add_f32 v176, v176, v178\n"
"v_mfma_f32_16x16x32_bf16 v[76:79], v[104:107], a[116:119], v[76:79]\n"
"s_add_u32 m0, s13, 16384\n"
"s_add_u32 s18, s7, 16384\n"
"buffer_load_dwordx4 v1, s[40:43], s18 offen offset:0 sc1 lds\n"
"s_waitcnt lgkmcnt(2)\n"
"v_mfma_f32_16x16x32_bf16 v[56:59], v[108:111], a[120:123], v[56:59]\n"
"ds_read_b128 v[96:99], v3 offset:12288\n"
"v_mfma_f32_16x16x32_bf16 v[60:63], v[108:111], a[124:127], v[60:63]\n"
"ds_read_b128 v[100:103], v3 offset:13312\n"
"s_waitcnt lgkmcnt(3)\n"
"v_mfma_f32_16x16x32_bf16 v[64:67], v[112:115], a[120:123], v[64:67]\n"
"ds_read_b128 v[104:107], v229 offset:40960\n"
"v_mfma_f32_16x16x32_bf16 v[68:71], v[112:115], a[124:127], v[68:71]\n"
"s_waitcnt lgkmcnt(3)\n"
"v_mfma_f32_16x16x32_bf16 v[72:75], v[116:119], a[120:123], v[72:75]\n"
"s_waitcnt vmcnt(19)\n"
"v_add_f32 v176, v176, v179\n"
"s_waitcnt vmcnt(17)\n"
"v_add_f32 v176, v176, v180\n"
"v_mfma_f32_16x16x32_bf16 v[76:79], v[116:119], a[124:127], v[76:79]\n"
"buffer_load_dwordx4 v1, s[40:43], s18 offen offset:1024 sc1 lds\n"
"s_waitcnt lgkmcnt(2)\n"
"v_mfma_f32_16x16x32_bf16 v[56:59], v[96:99], a[128:131], v[56:59]\n"
"ds_read_b128 v[108:111], v3 offset:14848\n"
"v_mfma_f32_16x16x32_bf16 v[60:63], v[96:99], a[132:135], v[60:63]\n"
"ds_read_b128 v[112:115], v3 offset:15872\n"
"s_waitcnt lgkmcnt(3)\n"
"v_mfma_f32_16x16x32_bf16 v[64:67], v[100:103], a[128:131], v[64:67]\n"
"ds_read_b128 v[116:119], v229 offset:43520\n"
"v_mfma_f32_16x16x32_bf16 v[68:71], v[100:103], a[132:135], v[68:71]\n"
"buffer_load_dwordx4 v1, s[40:43], s18 offen offset:2048 sc1 lds\n"
"s_waitcnt lgkmcnt(3)\n"
"v_mfma_f32_16x16x32_bf16 v[72:75], v[104:107], a[128:131], v[72:75]\n"
"s_waitcnt vmcnt(17)\n"
"v_add_f32 v176, v176, v181\n"
"s_waitcnt vmcnt(15)\n"
"v_add_f32 v176, v176, v182\n"
"v_mfma_f32_16x16x32_bf16 v[76:79], v[104:107], a[132:135], v[76:79]\n"
"s_waitcnt lgkmcnt(2)\n"
"v_mfma_f32_16x16x32_bf16 v[56:59], v[108:111], a[136:139], v[56:59]\n"
"ds_read_b128 v[96:99], v3 offset:17408\n"
"v_mfma_f32_16x16x32_bf16 v[60:63], v[108:111], a[140:143], v[60:63]\n"
"ds_read_b128 v[100:103], v3 offset:18432\n"
"s_waitcnt lgkmcnt(3)\n"
"v_mfma_f32_16x16x32_bf16 v[64:67], v[112:115], a[136:139], v[64:67]\n"
"ds_read_b128 v[104:107], v229 offset:46080\n"
"v_mfma_f32_16x16x32_bf16 v[68:71], v[112:115], a[140:143], v[68:71]\n"
"buffer_load_dwordx4 v1, s[40:43], s18 offen offset:3072 sc1 lds\n"
"s_waitcnt lgkmcnt(3)\n"
"v_mfma_f32_16x16x32_bf16 v[72:75], v[116:119], a[136:139], v[72:75]\n"
"s_waitcnt vmcnt(14)\n"
"v_add_f32 v176, v176, v183\n"
"s_waitcnt vmcnt(12)\n"
"v_add_f32 v176, v176, v184\n"
"v_mfma_f32_16x16x32_bf16 v[76:79], v[116:119], a[140:143], v[76:79]\n"
"s_waitcnt lgkmcnt(2)\n"
"v_mfma_f32_16x16x32_bf16 v[56:59], v[96:99], a[144:147], v[56:59]\n"
"ds_read_b128 v[108:111], v3 offset:19968\n"
"v_mfma_f32_16x16x32_bf16 v[60:63], v[96:99], a[148:151], v[60:63]\n"
"ds_read_b128 v[112:115], v3 offset:20992\n"
"s_waitcnt lgkmcnt(3)\n"
"v_mfma_f32_16x16x32_bf16 v[64:67], v[100:103], a[144:147], v[64:67]\n"
"ds_read_b128 v[116:119], v229 offset:48640\n"
"v_mfma_f32_16x16x32_bf16 v[68:71], v[100:103], a[148:151], v[68:71]\n"
"s_add_u32 m0, s13, 32768\n"
"s_add_u32 s18, s7, 32768\n"
"buffer_load_dwordx4 v1, s[40:43], s18 offen offset:0 sc1 lds\n"
"s_waitcnt lgkmcnt(3)\n"
"v_mfma_f32_16x16x32_bf16 v[72:75], v[104:107], a[144:147], v[72:75]\n"
"s_waitcnt vmcnt(11)\n"
"v_add_f32 v176, v176, v185\n"
"v_mov_b32 v177, v176\n"
"v_mfma_f32_16x16x32_bf16 v[76:79], v[104:107], a[148:151], v[76:79]\n"
"s_mov_b64 exec, 0xffff\n"
"buffer_load_dwordx2 v[22:23], v10, s[40:43], s15 offen sc1\n"
"s_mov_b64 exec, -1\n"
"s_waitcnt lgkmcnt(2)\n"
"v_mfma_f32_16x16x32_bf16 v[56:59], v[108:111], a[152:155], v[56:59]\n"
"v_mfma_f32_16x16x32_bf16 v[60:63], v[108:111], a[156:159], v[60:63]\n"
"s_waitcnt lgkmcnt(1)\n"
"v_mfma_f32_16x16x32_bf16 v[64:67], v[112:115], a[152:155], v[64:67]\n"
"v_mfma_f32_16x16x32_bf16 v[68:71], v[112:115], a[156:159], v[68:71]\n"
"s_waitcnt lgkmcnt(0)\n"
"v_mfma_f32_16x16x32_bf16 v[72:75], v[116:119], a[152:155], v[72:75]\n"
"v_permlane32_swap_b32 v176, v177\n"
"v_add_f32 v176, v176, v177\n"
"ds_write_b32 v231, v176 offset:7168\n"
"v_mfma_f32_16x16x32_bf16 v[76:79], v[116:119], a[156:159], v[76:79]\n"
"s_mul_i32 s58, s53, 61440\n"
"s_add_u32 s58, s58, 8372224\n"
"v_pk_fma_f32 v[56:57], v[44:45], v[16:17], v[56:57] op_sel:[0,0,0] op_sel_hi:[1,0,1]\n"
"v_pk_fma_f32 v[60:61], v[44:45], v[16:17], v[60:61] op_sel:[0,1,0] op_sel_hi:[1,1,1]\n"
"v_pk_fma_f32 v[58:59], v[46:47], v[16:17], v[58:59] op_sel:[0,0,0] op_sel_hi:[1,0,1]\n"
"v_pk_fma_f32 v[62:63], v[46:47], v[16:17], v[62:63] op_sel:[0,1,0] op_sel_hi:[1,1,1]\n"
"buffer_load_dwordx4 v1, s[40:43], s18 offen offset:1024 sc1 lds\n"
"v_pk_fma_f32 v[64:65], v[48:49], v[16:17], v[64:65] op_sel:[0,0,0] op_sel_hi:[1,0,1]\n"
"v_pk_fma_f32 v[68:69], v[48:49], v[16:17], v[68:69] op_sel:[0,1,0] op_sel_hi:[1,1,1]\n"
"v_pk_fma_f32 v[66:67], v[50:51], v[16:17], v[66:67] op_sel:[0,0,0] op_sel_hi:[1,0,1]\n"
"v_pk_fma_f32 v[70:71], v[50:51], v[16:17], v[70:71] op_sel:[0,1,0] op_sel_hi:[1,1,1]\n"
"v_pk_fma_f32 v[72:73], v[52:53], v[16:17], v[72:73] op_sel:[0,0,0] op_sel_hi:[1,0,1]\n"
"v_pk_fma_f32 v[76:77], v[52:53], v[16:17], v[76:77] op_sel:[0,1,0] op_sel_hi:[1,1,1]\n"
"v_max3_f32 v136, v56, v57, v58\n"
"v_max3_f32 v138, v60, v61, v62\n"
"buffer_load_dwordx4 v1, s[40:43], s18 offen offset:2048 sc1 lds\n"
"v_max3_f32 v137, -v56, -v57, -v58\n"
"v_max3_f32 v139, -v60, -v61, -v62\n"
"v_max3_f32 v136, v136, v59, v64\n"
"v_max3_f32 v138, v138, v63, v68\n"
"v_max3_f32 v137, v137, -v59, -v64\n"
"v_max3_f32 v139, v139, -v63, -v68\n"
"v_max3_f32 v136, v136, v65, v66\n"
"v_max3_f32 v138, v138, v69, v70\n"
"buffer_load_dwordx4 v1, s[40:43], s18 offen offset:3072 sc1 lds\n"
"v_max3_f32 v137, v137, -v65, -v66\n"
"v_max3_f32 v139, v139, -v69, -v70\n"
"v_max3_f32 v136, v136, v67, v72\n"
"v_max3_f32 v138, v138, v71, v76\n"
"v_max3_f32 v137, v137, -v67, -v72\n"
"v_max3_f32 v139, v139, -v71, -v76\n"
"v_max_f32 v136, v136, v73\n"
"v_max_f32 v138, v138, v77\n"
"s_add_u32 m0, s13, 49152\n"
"s_add_u32 s18, s7, 49152\n"
"buffer_load_dwordx4 v1, s[40:43], s18 offen offset:0 sc1 lds\n"
"v_max_f32 v137, v137, -v73\n"
"v_max_f32 v139, v139, -v77\n"
"s_nop 0\n"
"v_permlane16_swap_b32 v136, v137\n"
"v_permlane16_swap_b32 v138, v139\n"
"v_max_f32 v136, v136, v137\n"
"v_max_f32 v138, v138, v139\n"
"v_mov_b32 v137, v136\n"
"v_mov_b32 v139, v138\n"
"s_nop 0\n"
"v_permlane16_swap_b32 v136, v137\n"
"v_permlane16_swap_b32 v138, v139\n"
"v_pk_mul_f32 v[136:137], v[136:137], v[20:21] op_sel:[0,0] op_sel_hi:[1,0] neg_lo:[1,0] neg_hi:[1,0]\n"
"v_pk_mul_f32 v[138:139], v[138:139], v[20:21] op_sel:[0,1] op_sel_hi:[1,1] neg_lo:[1,0] neg_hi:[1,0]\n"
"s_mov_b64 exec, 0xffff\n"
"s_mov_b32 s67, 262144\n"
"s_waitcnt vmcnt(4)\n"
"v_cmp_ne_u64 vcc, s[56:57], v[22:23]\n"
"s_nop 3\n"
"s_cbranch_vccz PD17_%=\n"
"PL16_%=:\n"
"buffer_load_dwordx2 v[22:23], v10, s[40:43], s15 offen sc1\n"
"s_waitcnt vmcnt(0)\n"
"v_cmp_ne_u64 vcc, s[56:57], v[22:23]\n"
"s_nop 3\n"
"s_cbranch_vccz PD17_%=\n"
"s_sub_u32 s67, s67, 1\n"
"s_cbranch_scc0 PL16_%=\n"
"PD17_%=:\n"
"s_mov_b64 exec, -1\n"
"buffer_load_dwordx4 v[160:163], v235, s[40:43], s58 offen sc1\n"
"s_add_u32 s58, s58, 3840\n"
"buffer_load_dwordx4 v[164:167], v235, s[40:43], s58 offen sc1\n"
"s_add_u32 s58, s58, 3840\n"
"buffer_load_dwordx4 v[168:171], v235, s[40:43], s58 offen sc1\n"
"s_add_u32 s58, s58, 3840\n"
"buffer_load_dwordx4 v[172:175], v235, s[40:43], s58 offen sc1\n"
"s_add_u32 s58, s58, 3840\n"
"v_pk_fma_f32 v[96:97], v[56:57], v[20:21], v[136:137] op_sel:[0,0,0] op_sel_hi:[1,0,0]\n"
"v_pk_fma_f32 v[116:117], v[60:61], v[20:21], v[138:139] op_sel:[0,1,0] op_sel_hi:[1,1,0]\n"
"v_pk_fma_f32 v[106:107], v[56:57], v[20:21], v[136:137] op_sel:[0,0,1] op_sel_hi:[1,0,1] neg_lo:[1,0,0] neg_hi:[1,0,0]\n"
"v_pk_fma_f32 v[126:127], v[60:61], v[20:21], v[138:139] op_sel:[0,1,1] op_sel_hi:[1,1,1] neg_lo:[1,0,0] neg_hi:[1,0,0]\n"
"buffer_load_dwordx4 v[176:179], v235, s[40:43], s58 offen sc1\n"
"s_add_u32 s58, s58, 3840\n"
"v_pk_fma_f32 v[98:99], v[58:59], v[20:21], v[136:137] op_sel:[0,0,0] op_sel_hi:[1,0,0]\n"
"v_pk_fma_f32 v[118:119], v[62:63], v[20:21], v[138:139] op_sel:[0,1,0] op_sel_hi:[1,1,0]\n"
"v_pk_fma_f32 v[108:109], v[58:59], v[20:21], v[136:137] op_sel:[0,0,1] op_sel_hi:[1,0,1] neg_lo:[1,0,0] neg_hi:[1,0,0]\n"
"v_pk_fma_f32 v[128:129], v[62:63], v[20:21], v[138:139] op_sel:[0,1,1] op_sel_hi:[1,1,1] neg_lo:[1,0,0] neg_hi:[1,0,0]\n"
"buffer_load_dwordx4 v[180:183], v235, s[40:43], s58 offen sc1\n"
"s_add_u32 s58, s58, 3840\n"
"v_pk_fma_f32 v[100:101], v[64:65], v[20:21], v[136:137] op_sel:[0,0,0] op_sel_hi:[1,0,0]\n"
"v_pk_fma_f32 v[120:121], v[68:69], v[20:21], v[138:139] op_sel:[0,1,0] op_sel_hi:[1,1,0]\n"
"v_pk_fma_f32 v[110:111], v[64:65], v[20:21], v[136:137] op_sel:[0,0,1] op_sel_hi:[1,0,1] neg_lo:[1,0,0] neg_hi:[1,0,0]\n"
"v_pk_fma_f32 v[130:131], v[68:69], v[20:21], v[138:139] op_sel:[0,1,1] op_sel_hi:[1,1,1] neg_lo:[1,0,0] neg_hi:[1,0,0]\n"
"buffer_load_dwordx4 v[184:187], v235, s[40:43], s58 offen sc1\n"
"s_add_u32 s58, s58, 3840\n"
"v_pk_fma_f32 v[102:103], v[66:67], v[20:21], v[136:137] op_sel:[0,0,0] op_sel_hi:[1,0,0]\n"
"v_pk_fma_f32 v[122:123], v[70:71], v[20:21], v[138:139] op_sel:[0,1,0] op_sel_hi:[1,1,0]\n"
"v_pk_fma_f32 v[112:113], v[66:67], v[20:21], v[136:137] op_sel:[0,0,1] op_sel_hi:[1,0,1] neg_lo:[1,0,0] neg_hi:[1,0,0]\n"
"v_pk_fma_f32 v[132:133], v[70:71], v[20:21], v[138:139] op_sel:[0,1,1] op_sel_hi:[1,1,1] neg_lo:[1,0,0] neg_hi:[1,0,0]\n"
"buffer_load_dwordx4 v[188:191], v235, s[40:43], s58 offen sc1\n"
"s_add_u32 s58, s58, 3840\n"
"v_pk_fma_f32 v[104:105], v[72:73], v[20:21], v[136:137] op_sel:[0,0,0] op_sel_hi:[1,0,0]\n"
"v_pk_fma_f32 v[124:125], v[76:77], v[20:21], v[138:139] op_sel:[0,1,0] op_sel_hi:[1,1,0]\n"
"v_pk_fma_f32 v[114:115], v[72:73], v[20:21], v[136:137] op_sel:[0,0,1] op_sel_hi:[1,0,1] neg_lo:[1,0,0] neg_hi:[1,0,0]\n"
"v_pk_fma_f32 v[134:135], v[76:77], v[20:21], v[138:139] op_sel:[0,1,1] op_sel_hi:[1,1,1] neg_lo:[1,0,0] neg_hi:[1,0,0]\n"
"buffer_load_dwordx4 v[192:195], v235, s[40:43], s58 offen sc1\n"
"s_add_u32 s58, s58, 3840\n"
"v_exp_f32 v96, v96\n"
"v_exp_f32 v116, v116\n"
"v_exp_f32 v106, v106\n"
"v_exp_f32 v126, v126\n"
"buffer_load_dwordx4 v[196:199], v235, s[40:43], s58 offen sc1\n"
"s_add_u32 s58, s58, 3840\n"
"v_exp_f32 v97, v97\n"
"v_exp_f32 v117, v117\n"
"v_exp_f32 v107, v107\n"
"v_exp_f32 v127, v127\n"
"buffer_load_dwordx4 v[200:203], v235, s[40:43], s58 offen sc1\n"
"s_add_u32 s58, s58, 3840\n"
"v_exp_f32 v98, v98\n"
"v_exp_f32 v118, v118\n"
"v_exp_f32 v108, v108\n"
"v_exp_f32 v128, v128\n"
"buffer_load_dwordx4 v[204:207], v235, s[40:43], s58 offen sc1\n"
"s_add_u32 s58, s58, 3840\n"
"v_exp_f32 v99, v99\n"
"v_exp_f32 v119, v119\n"
"v_exp_f32 v109, v109\n"
"v_exp_f32 v129, v129\n"
"buffer_load_dwordx4 v[208:211], v235, s[40:43], s58 offen sc1\n"
"s_add_u32 s58, s58, 3840\n"
"v_exp_f32 v100, v100\n"
"v_exp_f32 v120, v120\n"
"v_exp_f32 v110, v110\n"
"v_exp_f32 v130, v130\n"
"buffer_load_dwordx4 v[212:215], v235, s[40:43], s58 offen sc1\n"
"s_add_u32 s58, s58, 3840\n"
"v_exp_f32 v101, v101\n"
"v_exp_f32 v121, v121\n"
"v_exp_f32 v111, v111\n"
"v_exp_f32 v131, v131\n"
"buffer_load_dwordx4 v[216:219], v235, s[40:43], s58 offen sc1\n"
"s_add_u32 s58, s58, 3840\n"
"v_exp_f32 v102, v102\n"
"v_exp_f32 v122, v122\n"
"v_exp_f32 v112, v112\n"
"v_exp_f32 v132, v132\n"
"buffer_load_dwordx4 v[220:223], v235, s[40:43], s58 offen sc1\n"
"s_add_u32 s58, s58, 3840\n"
"v_exp_f32 v103, v103\n"
"v_exp_f32 v123, v123\n"
"v_exp_f32 v113, v113\n"
"v_exp_f32 v133, v133\n"
"buffer_load_dwordx4 v1, s[40:43], s18 offen offset:1024 sc1 lds\n"
"v_exp_f32 v104, v104\n"
"v_exp_f32 v124, v124\n"
"v_exp_f32 v114, v114\n"
"v_exp_f32 v134, v134\n"
"buffer_load_dwordx4 v1, s[40:43], s18 offen offset:2048 sc1 lds\n"
"v_exp_f32 v105, v105\n"
"v_exp_f32 v125, v125\n"
"v_exp_f32 v115, v115\n"
"v_exp_f32 v135, v135\n"
"buffer_load_dwordx4 v1, s[40:43], s18 offen offset:3072 sc1 lds\n"
"v_pk_add_f32 v[56:57], v[96:97], v[98:99]\n"
"v_pk_add_f32 v[60:61], v[116:117], v[118:119]\n"
"v_pk_add_f32 v[58:59], v[106:107], v[108:109]\n"
"v_pk_add_f32 v[62:63], v[126:127], v[128:129]\n"
"s_add_u32 m0, s13, 65536\n"
"s_add_u32 s18, s7, 65536\n"
"buffer_load_dwordx4 v1, s[40:43], s18 offen offset:0 sc1 lds\n"
"v_pk_add_f32 v[64:65], v[100:101], v[102:103]\n"
"v_pk_add_f32 v[68:69], v[120:121], v[122:123]\n"
"v_pk_add_f32 v[66:67], v[110:111], v[112:113]\n"
"v_pk_add_f32 v[70:71], v[130:131], v[132:133]\n"
"v_pk_add_f32 v[56:57], v[56:57], v[104:105]\n"
"v_pk_add_f32 v[60:61], v[60:61], v[124:125]\n"
"v_pk_add_f32 v[58:59], v[58:59], v[114:115]\n"
"v_pk_add_f32 v[62:63], v[62:63], v[134:135]\n"
"v_pk_add_f32 v[56:57], v[56:57], v[64:65]\n"
"v_pk_add_f32 v[60:61], v[60:61], v[68:69]\n"
"v_pk_add_f32 v[58:59], v[58:59], v[66:67]\n"
"v_pk_add_f32 v[62:63], v[62:63], v[70:71]\n"
"v_add_f32 v136, v56, v57\n"
"v_add_f32 v138, v60, v61\n"
"v_add_f32 v137, v58, v59\n"
"v_add_f32 v139, v62, v63\n"
"s_nop 0\n"
"v_permlane16_swap_b32 v136, v137\n"
"v_permlane16_swap_b32 v138, v139\n"
"v_add_f32 v136, v136, v137\n"
"v_add_f32 v138, v138, v139\n"
"v_rcp_f32 v136, v136\n"
"v_rcp_f32 v138, v138\n"
"v_mov_b32 v137, v136\n"
"v_mov_b32 v139, v138\n"
"s_nop 0\n"
"v_permlane16_swap_b32 v136, v137\n"
"v_permlane16_swap_b32 v138, v139\n"
"v_pk_mul_f32 v[96:97], v[96:97], v[136:137] op_sel:[0,0] op_sel_hi:[1,0]\n"
"v_pk_mul_f32 v[116:117], v[116:117], v[138:139] op_sel:[0,0] op_sel_hi:[1,0]\n"
"v_pk_mul_f32 v[106:107], v[106:107], v[136:137] op_sel:[0,1] op_sel_hi:[1,1]\n"
"v_pk_mul_f32 v[126:127], v[126:127], v[138:139] op_sel:[0,1] op_sel_hi:[1,1]\n"
"v_pk_mul_f32 v[98:99], v[98:99], v[136:137] op_sel:[0,0] op_sel_hi:[1,0]\n"
"v_pk_mul_f32 v[118:119], v[118:119], v[138:139] op_sel:[0,0] op_sel_hi:[1,0]\n"
"v_pk_mul_f32 v[108:109], v[108:109], v[136:137] op_sel:[0,1] op_sel_hi:[1,1]\n"
"v_pk_mul_f32 v[128:129], v[128:129], v[138:139] op_sel:[0,1] op_sel_hi:[1,1]\n"
"v_pk_mul_f32 v[100:101], v[100:101], v[136:137] op_sel:[0,0] op_sel_hi:[1,0]\n"
"v_pk_mul_f32 v[120:121], v[120:121], v[138:139] op_sel:[0,0] op_sel_hi:[1,0]\n"
"v_pk_mul_f32 v[110:111], v[110:111], v[136:137] op_sel:[0,1] op_sel_hi:[1,1]\n"
"v_pk_mul_f32 v[130:131], v[130:131], v[138:139] op_sel:[0,1] op_sel_hi:[1,1]\n"
"v_pk_mul_f32 v[102:103], v[102:103], v[136:137] op_sel:[0,0] op_sel_hi:[1,0]\n"
"v_pk_mul_f32 v[122:123], v[122:123], v[138:139] op_sel:[0,0] op_sel_hi:[1,0]\n"
"v_pk_mul_f32 v[112:113], v[112:113], v[136:137] op_sel:[0,1] op_sel_hi:[1,1]\n"
"v_pk_mul_f32 v[132:133], v[132:133], v[138:139] op_sel:[0,1] op_sel_hi:[1,1]\n"
"v_pk_mul_f32 v[104:105], v[104:105], v[136:137] op_sel:[0,0] op_sel_hi:[1,0]\n"
"v_pk_mul_f32 v[124:125], v[124:125], v[138:139] op_sel:[0,0] op_sel_hi:[1,0]\n"
"v_pk_mul_f32 v[114:115], v[114:115], v[136:137] op_sel:[0,1] op_sel_hi:[1,1]\n"
"v_pk_mul_f32 v[134:135], v[134:135], v[138:139] op_sel:[0,1] op_sel_hi:[1,1]\n"
"v_cvt_pk_bf16_f32 v140, v96, v97\n"
"v_cvt_pk_bf16_f32 v150, v116, v117\n"
"v_cvt_pk_bf16_f32 v144, v106, v107\n"
"v_cvt_pk_bf16_f32 v154, v126, v127\n"
"v_cvt_pk_bf16_f32 v141, v98, v99\n"
"v_cvt_pk_bf16_f32 v151, v118, v119\n"
"v_cvt_pk_bf16_f32 v145, v108, v109\n"
"v_cvt_pk_bf16_f32 v155, v128, v129\n"
"v_cvt_pk_bf16_f32 v142, v100, v101\n"
"v_cvt_pk_bf16_f32 v152, v120, v121\n"
"v_cvt_pk_bf16_f32 v146, v110, v111\n"
"v_cvt_pk_bf16_f32 v156, v130, v131\n"
"v_cvt_pk_bf16_f32 v143, v102, v103\n"
"v_cvt_pk_bf16_f32 v153, v122, v123\n"
"v_cvt_pk_bf16_f32 v147, v112, v113\n"
"v_cvt_pk_bf16_f32 v157, v132, v133\n"
"v_cvt_pk_bf16_f32 v148, v104, v105\n"
"v_cvt_pk_bf16_f32 v158, v124, v125\n"
"PAD4_%=:\n"
"s_cbranch_execz PADRET_%=\n"
"v_cvt_pk_bf16_f32 v149, v114, v115\n"
"v_cvt_pk_bf16_f32 v159, v134, v135\n"
"s_waitcnt vmcnt(19)\n"
"v_mfma_f32_16x16x32_bf16 v[88:91], v[80:83], v[160:163], 0\n"
"v_lshlrev_b32 v24, 16, v238\n"
"v_and_b32 v25, 0xffff0000, v238\n"
"v_mfma_f32_16x16x32_bf16 v[92:95], v[84:87], v[160:163], 0\n"
"v_lshlrev_b32 v26, 16, v239\n"
"v_and_b32 v27, 0xffff0000, v239\n"
"s_waitcnt vmcnt(18)\n"
"v_mfma_f32_16x16x32_bf16 v[88:91], v[80:83], v[164:167], v[88:91]\n"
"v_lshlrev_b32 v28, 16, v240\n"
"v_and_b32 v29, 0xffff0000, v240\n"
"v_mfma_f32_16x16x32_bf16 v[92:95], v[84:87], v[164:167], v[92:95]\n"
"v_lshlrev_b32 v30, 16, v241\n"
"v_and_b32 v31, 0xffff0000, v241\n"
"s_waitcnt vmcnt(17)\n"
"v_mfma_f32_16x16x32_bf16 v[88:91], v[80:83], v[168:171], v[88:91]\n"
"v_lshlrev_b32 v32, 16, v242\n"
"v_and_b32 v33, 0xffff0000, v242\n"
"v_mfma_f32_16x16x32_bf16 v[92:95], v[84:87], v[168:171], v[92:95]\n"
"buffer_load_dwordx4 v1, s[40:43], s18 offen offset:1024 sc1 lds\n"
"s_waitcnt vmcnt(17)\n"
"v_mfma_f32_16x16x32_bf16 v[88:91], v[80:83], v[172:175], v[88:91]\n"
"v_lshlrev_b32 v34, 16, v243\n"
"v_and_b32 v35, 0xffff0000, v243\n"
"v_mfma_f32_16x16x32_bf16 v[92:95], v[84:87], v[172:175], v[92:95]\n"
"v_lshlrev_b32 v36, 16, v244\n"
"v_and_b32 v37, 0xffff0000, v244\n"
"s_waitcnt vmcnt(16)\n"
"v_mfma_f32_16x16x32_bf16 v[88:91], v[80:83], v[176:179], v[88:91]\n"
"v_lshlrev_b32 v38, 16, v245\n"
"v_and_b32 v39, 0xffff0000, v245\n"
"v_mfma_f32_16x16x32_bf16 v[92:95], v[84:87], v[176:179], v[92:95]\n"
"v_lshlrev_b32 v40, 16, v246\n"
"v_and_b32 v41, 0xffff0000, v246\n"
"s_waitcnt vmcnt(15)\n"
"v_mfma_f32_16x16x32_bf16 v[88:91], v[80:83], v[180:183], v[88:91]\n"
"buffer_load_dwordx4 v1, s[40:43], s18 offen offset:2048 sc1 lds\n"
"v_mfma_f32_16x16x32_bf16 v[92:95], v[84:87], v[180:183], v[92:95]\n"
"v_lshlrev_b32 v42, 16, v247\n"
"v_and_b32 v43, 0xffff0000, v247\n"
"s_waitcnt vmcnt(15)\n"
"v_mfma_f32_16x16x32_bf16 v[88:91], v[80:83], v[184:187], v[88:91]\n"
"v_mfma_f32_16x16x32_bf16 v[92:95], v[84:87], v[184:187], v[92:95]\n"
"s_waitcnt vmcnt(14)\n"
"v_mfma_f32_16x16x32_bf16 v[88:91], v[80:83], v[188:191], v[88:91]\n"
"v_mfma_f32_16x16x32_bf16 v[92:95], v[84:87], v[188:191], v[92:95]\n"
"s_waitcnt vmcnt(13)\n"
"v_mfma_f32_16x16x32_bf16 v[88:91], v[80:83], v[192:195], v[88:91]\n"
"buffer_load_dwordx4 v1, s[40:43], s18 offen offset:3072 sc1 lds\n"
"v_mfma_f32_16x16x32_bf16 v[92:95], v[84:87], v[192:195], v[92:95]\n"
"s_waitcnt vmcnt(13)\n"
"v_mfma_f32_16x16x32_bf16 v[88:91], v[80:83], v[196:199], v[88:91]\n"
"v_mfma_f32_16x16x32_bf16 v[92:95], v[84:87], v[196:199], v[92:95]\n"
"s_waitcnt vmcnt(12)\n"
"v_mfma_f32_16x16x32_bf16 v[88:91], v[80:83], v[200:203], v[88:91]\n"
"v_mfma_f32_16x16x32_bf16 v[92:95], v[84:87], v[200:203], v[92:95]\n"
"s_add_u32 m0, s13, 81920\n"
"s_add_u32 s18, s7, 81920\n"
"buffer_load_dwordx4 v1, s[40:43], s18 offen offset:0 sc1 lds\n"
"s_waitcnt vmcnt(12)\n"
"v_mfma_f32_16x16x32_bf16 v[88:91], v[80:83], v[204:207], v[88:91]\n"
"v_mfma_f32_16x16x32_bf16 v[92:95], v[84:87], v[204:207], v[92:95]\n"
"s_waitcnt vmcnt(11)\n"
"v_mfma_f32_16x16x32_bf16 v[88:91], v[80:83], v[208:211], v[88:91]\n"
"v_mfma_f32_16x16x32_bf16 v[92:95], v[84:87], v[208:211], v[92:95]\n"
"s_waitcnt vmcnt(10)\n"
"v_mfma_f32_16x16x32_bf16 v[88:91], v[80:83], v[212:215], v[88:91]\n"
"buffer_load_dwordx4 v1, s[40:43], s18 offen offset:1024 sc1 lds\n"
"v_mfma_f32_16x16x32_bf16 v[92:95], v[84:87], v[212:215], v[92:95]\n"
"s_waitcnt vmcnt(10)\n"
"v_mfma_f32_16x16x32_bf16 v[88:91], v[80:83], v[216:219], v[88:91]\n"
"v_mfma_f32_16x16x32_bf16 v[92:95], v[84:87], v[216:219], v[92:95]\n"
"s_waitcnt vmcnt(9)\n"
"v_mfma_f32_16x16x32_bf16 v[88:91], v[80:83], v[220:223], v[88:91]\n"
"v_mfma_f32_16x16x32_bf16 v[92:95], v[84:87], v[220:223], v[92:95]\n"
"buffer_load_dwordx4 v1, s[40:43], s18 offen offset:2048 sc1 lds\n"
"buffer_load_dwordx4 v1, s[40:43], s18 offen offset:3072 sc1 lds\n"
"s_nop 4\n"
"v_cvt_pk_bf16_f32 v88, v88, v89\n"
"v_cvt_pk_bf16_f32 v89, v90, v91\n"
"v_cvt_pk_bf16_f32 v90, v92, v93\n"
"v_cvt_pk_bf16_f32 v91, v94, v95\n"
"ds_write_b64 v236, v[88:89] offset:0\n"
"ds_write_b64 v236, v[90:91] offset:512\n"
"s_add_u32 s46, s21, 24576\n"
"buffer_load_dwordx4 a[0:3], v1, s[40:43], s46 offen offset:1024 sc1\n"
"s_waitcnt lgkmcnt(0)\n"
"s_barrier\n"
"ds_read_b128 v[96:99], v233 offset:0\n"
"ds_read_b128 v[108:111], v233 offset:320\n"
"ds_read_b128 v[120:123], v233 offset:640\n"
"ds_read_b128 v[100:103], v233 offset:32\n"
"ds_read_b128 v[112:115], v233 offset:352\n"
"ds_read_b128 v[124:127], v233 offset:672\n"
"ds_read_b64 v[104:105], v234 offset:0\n"
"ds_read_b64 v[116:117], v234 offset:320\n"
"ds_read_b64 v[128:129], v234 offset:640\n"
"s_lshl_b32 s19, s52, 3\n"
"s_lshl_b32 s58, s49, 1\n"
"s_add_u32 s19, s19, s58\n"
"s_mul_i32 s19, s19, 20480\n"
"s_add_u32 s19, s19, 9359360\n"
"s_lshr_b32 s58, s53, 2\n"
"s_lshl_b32 s58, s58, 10\n"
"s_and_b32 s59, s53, 3\n"
"s_lshl_b32 s59, s59, 2\n"
"s_add_u32 s58, s58, s59\n"
"s_add_u32 s58, s58, 16384\n"
"s_add_u32 s58, s58, s19\n"
"s_lshl_b32 s59, s53, 10\n"
"s_add_u32 s19, s19, s59\n"
"s_waitcnt lgkmcnt(8)\n"
"v_mfma_f32_16x16x32_bf16 v[56:59], v[96:99], v[140:143], 0\n"
"s_waitcnt lgkmcnt(7)\n"
"v_mfma_f32_16x16x32_bf16 v[60:63], v[108:111], v[140:143], 0\n"
"s_waitcnt lgkmcnt(6)\n"
"v_mfma_f32_16x16x32_bf16 v[64:67], v[120:123], v[140:143], 0\n"
"v_mfma_f32_16x16x32_bf16 v[68:71], v[96:99], v[150:153], 0\n"
"v_mfma_f32_16x16x32_bf16 v[72:75], v[108:111], v[150:153], 0\n"
"v_mfma_f32_16x16x32_bf16 v[76:79], v[120:123], v[150:153], 0\n"
"s_waitcnt lgkmcnt(5)\n"
"v_mfma_f32_16x16x32_bf16 v[56:59], v[100:103], v[144:147], v[56:59]\n"
"s_waitcnt lgkmcnt(4)\n"
"v_mfma_f32_16x16x32_bf16 v[60:63], v[112:115], v[144:147], v[60:63]\n"
"s_waitcnt lgkmcnt(3)\n"
"v_mfma_f32_16x16x32_bf16 v[64:67], v[124:127], v[144:147], v[64:67]\n"
"v_mfma_f32_16x16x32_bf16 v[68:71], v[100:103], v[154:157], v[68:71]\n"
"v_mfma_f32_16x16x32_bf16 v[72:75], v[112:115], v[154:157], v[72:75]\n"
"v_mfma_f32_16x16x32_bf16 v[76:79], v[124:127], v[154:157], v[76:79]\n"
"s_waitcnt lgkmcnt(2)\n"
"v_mfma_f32_16x16x16_bf16 v[56:59], v[104:105], v[148:149], v[56:59]\n"
"s_waitcnt lgkmcnt(1)\n"
"v_mfma_f32_16x16x16_bf16 v[60:63], v[116:117], v[148:149], v[60:63]\n"
"s_waitcnt lgkmcnt(0)\n"
"v_mfma_f32_16x16x16_bf16 v[64:67], v[128:129], v[148:149], v[64:67]\n"
"v_mfma_f32_16x16x16_bf16 v[68:71], v[104:105], v[158:159], v[68:71]\n"
"v_mfma_f32_16x16x16_bf16 v[72:75], v[116:117], v[158:159], v[72:75]\n"
"v_mfma_f32_16x16x16_bf16 v[76:79], v[128:129], v[158:159], v[76:79]\n"
"s_nop 4\n"
"v_add_f32 v66, 1e-06, v66\n"
"s_nop 1\n"
"v_add_f32 v78, 1e-06, v78\n"
"v_rcp_f32 v66, v66\n"
"v_rcp_f32 v78, v78\n"
"v_pk_mul_f32 v[56:57], v[56:57], v[66:67] op_sel:[0,0] op_sel_hi:[1,0]\n"
"v_pk_mul_f32 v[68:69], v[68:69], v[78:79] op_sel:[0,0] op_sel_hi:[1,0]\n"
"v_pk_mul_f32 v[58:59], v[58:59], v[66:67] op_sel:[0,0] op_sel_hi:[1,0]\n"
"v_pk_mul_f32 v[70:71], v[70:71], v[78:79] op_sel:[0,0] op_sel_hi:[1,0]\n"
"v_pk_mul_f32 v[60:61], v[60:61], v[66:67] op_sel:[0,0] op_sel_hi:[1,0]\n"
"v_pk_mul_f32 v[72:73], v[72:73], v[78:79] op_sel:[0,0] op_sel_hi:[1,0]\n"
"v_pk_mul_f32 v[62:63], v[62:63], v[66:67] op_sel:[0,0] op_sel_hi:[1,0]\n"
"v_pk_mul_f32 v[74:75], v[74:75], v[78:79] op_sel:[0,0] op_sel_hi:[1,0]\n"
"v_pk_mul_f32 v[64:65], v[64:65], v[66:67] op_sel:[0,0] op_sel_hi:[1,0]\n"
"v_pk_mul_f32 v[76:77], v[76:77], v[78:79] op_sel:[0,0] op_sel_hi:[1,0]\n"
"v_cvt_pk_bf16_f32 v132, v56, v57\n"
"v_cvt_pk_bf16_f32 v138, v68, v69\n"
"v_cvt_pk_bf16_f32 v133, v58, v59\n"
"v_cvt_pk_bf16_f32 v139, v70, v71\n"
"v_cvt_pk_bf16_f32 v134, v60, v61\n"
"v_cvt_pk_bf16_f32 v140, v72, v73\n"
"v_cvt_pk_bf16_f32 v135, v62, v63\n"
"v_cvt_pk_bf16_f32 v141, v74, v75\n"
"v_cvt_pk_bf16_f32 v136, v64, v65\n"
"v_cvt_pk_bf16_f32 v142, v76, v77\n"
"buffer_store_dwordx4 v[132:135], v1, s[40:43], s19 offen sc1\n"
"buffer_store_dword v136, v1, s[40:43], s58 offen sc1\n"
"s_add_u32 s19, s19, 20480\n"
"s_add_u32 s58, s58, 20480\n"
"buffer_store_dwordx4 v[138:141], v1, s[40:43], s19 offen sc1\n"
"buffer_store_dword v142, v1, s[40:43], s58 offen sc1\n"
"s_add_u32 s46, s21, 24576\n"
"buffer_load_dwordx4 a[4:7], v1, s[40:43], s46 offen offset:2048 sc1\n"
"s_lshl_b32 s69, s52, 11\n"
"s_lshl_b32 s67, s53, 3\n"
"s_add_u32 s69, s69, s67\n"
"s_add_u32 s69, s69, 102400\n"
"s_lshl_b32 s76, s54, 11\n"
"s_add_u32 s76, s76, 102400\n"
"s_sub_u32 s58, s24, s49\n"
"s_add_u32 s62, s49, 1\n"
"s_and_b32 s62, s62, 3\n"
"s_mul_i32 s63, s62, 15360\n"
"s_add_u32 s63, s63, 98304\n"
"v_add_u32 v12, s63, v1\n"
"s_add_u32 s62, s62, s58\n"
"s_mul_i32 s62, s62, 20\n"
"s_mul_i32 s63, s49, 5\n"
"s_add_u32 s62, s62, s63\n"
"s_lshl_b32 s62, s62, 10\n"
"s_add_u32 s90, s62, 9359360\n"
"s_add_u32 s62, s49, 2\n"
"s_and_b32 s62, s62, 3\n"
"s_add_u32 s46, s21, 24576\n"
"buffer_load_dwordx4 a[8:11], v1, s[40:43], s46 offen offset:3072 sc1\n"
"s_mul_i32 s63, s62, 15360\n"
"s_add_u32 s63, s63, 103424\n"
"v_add_u32 v13, s63, v1\n"
"s_add_u32 s62, s62, s58\n"
"s_mul_i32 s62, s62, 20\n"
"s_mul_i32 s63, s49, 5\n"
"s_add_u32 s62, s62, s63\n"
"s_lshl_b32 s62, s62, 10\n"
"s_add_u32 s91, s62, 9359360\n"
"s_add_u32 s62, s49, 3\n"
"s_and_b32 s62, s62, 3\n"
"s_mul_i32 s63, s62, 15360\n"
"s_add_u32 s63, s63, 108544\n"
"v_add_u32 v14, s63, v1\n"
"s_add_u32 s62, s62, s58\n"
"s_mul_i32 s62, s62, 20\n"
"s_mul_i32 s63, s49, 5\n"
"s_add_u32 s62, s62, s63\n"
"s_lshl_b32 s62, s62, 10\n"
"s_add_u32 s92, s62, 9359360\n"
"s_add_u32 s46, s21, 28672\n"
"buffer_load_dwordx4 a[12:15], v1, s[40:43], s46 offen offset:0 sc1\n"
"s_add_u32 s62, s49, 4\n"
"s_and_b32 s62, s62, 3\n"
"s_add_u32 s62, s62, s58\n"
"s_mul_i32 s62, s62, 20\n"
"s_mul_i32 s63, s49, 5\n"
"s_add_u32 s62, s62, s63\n"
"s_lshl_b32 s62, s62, 10\n"
"s_add_u32 s93, s62, 9359360\n"
"s_mul_i32 s63, s49, 15360\n"
"s_add_u32 s63, s63, 98304\n"
"v_add_u32 v15, s63, v1\n"
"v_add_u32 v19, 160768, v7\n"
"v_and_b32 v21, 1, v9\n"
"v_lshlrev_b32 v21, 2, v21\n"
"v_lshl_add_u32 v20, v8, 4, v21\n"
"s_lshr_b32 s62, s20, 1\n"
"s_and_b32 s63, s20, 1\n"
"s_lshl_b32 s72, s24, 2\n"
"s_add_u32 s72, s72, s62\n"
"s_lshl_b32 s72, s72, 5\n"
"s_add_u32 s46, s21, 28672\n"
"buffer_load_dwordx4 a[16:19], v1, s[40:43], s46 offen offset:1024 sc1\n"
"s_add_u32 s72, s72, s63\n"
"s_lshl_b32 s72, s72, 3\n"
"s_add_u32 s72, s72, 14602240\n"
"s_lshl_b32 s64, s24, 2\n"
"s_add_u32 s64, s64, s62\n"
"s_mul_i32 s64, s64, 5120\n"
"s_add_u32 s64, s64, 11980800\n"
"s_lshl_b32 s65, s63, 11\n"
"s_add_u32 s68, s64, s65\n"
"s_lshl_b32 s65, s63, 3\n"
"s_add_u32 s73, s64, s65\n"
"s_add_u32 s73, s73, 4096\n"
"s_lshl_b32 s70, s54, 11\n"
"s_lshl_b32 s67, s55, 3\n"
"s_add_u32 s70, s70, s67\n"
"s_add_u32 s70, s70, 135168\n"
"s_lshl_b32 s71, s54, 11\n"
"s_lshl_b32 s67, s53, 3\n"
"s_add_u32 s71, s71, s67\n"
"s_add_u32 s71, s71, 167936\n"
"s_waitcnt vmcnt(4)\n"
"s_barrier\n"
"s_cmp_lg_u32 s49, 0\n"
"s_cbranch_scc1 FS18_%=\n"
"s_mov_b64 exec, 1\n"
"buffer_store_dwordx2 v[4:5], off, s[40:43], s69 sc1\n"
"s_mov_b64 exec, -1\n"
"FS18_%=:\n"
"s_add_u32 s46, s21, 32768\n"
"buffer_load_dwordx4 a[20:23], v1, s[40:43], s46 offen offset:3072 sc1\n"
"s_add_u32 s46, s21, 36864\n"
"buffer_load_dwordx4 a[24:27], v1, s[40:43], s46 offen offset:0 sc1\n"
"s_add_u32 s46, s21, 36864\n"
"buffer_load_dwordx4 a[28:31], v1, s[40:43], s46 offen offset:1024 sc1\n"
"s_add_u32 s46, s21, 36864\n"
"buffer_load_dwordx4 a[32:35], v1, s[40:43], s46 offen offset:2048 sc1\n"
"s_add_u32 s46, s21, 36864\n"
"buffer_load_dwordx4 a[36:39], v1, s[40:43], s46 offen offset:3072 sc1\n"
"s_add_u32 s46, s21, 40960\n"
"buffer_load_dwordx4 a[40:43], v1, s[40:43], s46 offen offset:0 sc1\n"
"s_add_u32 s46, s21, 40960\n"
"buffer_load_dwordx4 a[44:47], v1, s[40:43], s46 offen offset:1024 sc1\n"
"s_add_u32 s46, s21, 40960\n"
"buffer_load_dwordx4 a[48:51], v1, s[40:43], s46 offen offset:2048 sc1\n"
"s_add_u32 s46, s21, 40960\n"
"buffer_load_dwordx4 a[52:55], v1, s[40:43], s46 offen offset:3072 sc1\n"
"s_add_u32 s46, s21, 45056\n"
"buffer_load_dwordx4 a[56:59], v1, s[40:43], s46 offen offset:0 sc1\n"
"s_add_u32 s46, s47, 53248\n"
"buffer_load_dwordx4 a[60:63], v1, s[40:43], s46 offen offset:3072 sc1\n"
"s_add_u32 s46, s47, 57344\n"
"buffer_load_dwordx4 a[64:67], v1, s[40:43], s46 offen offset:0 sc1\n"
"s_add_u32 s46, s47, 57344\n"
"buffer_load_dwordx4 a[68:71], v1, s[40:43], s46 offen offset:1024 sc1\n"
"s_add_u32 s46, s47, 57344\n"
"buffer_load_dwordx4 a[72:75], v1, s[40:43], s46 offen offset:2048 sc1\n"
"s_add_u32 s46, s47, 57344\n"
"buffer_load_dwordx4 a[76:79], v1, s[40:43], s46 offen offset:3072 sc1\n"
"s_add_u32 s46, s47, 61440\n"
"buffer_load_dwordx4 a[80:83], v1, s[40:43], s46 offen offset:0 sc1\n"
"s_add_u32 s46, s47, 61440\n"
"buffer_load_dwordx4 a[84:87], v1, s[40:43], s46 offen offset:1024 sc1\n"
"s_add_u32 s46, s47, 61440\n"
"buffer_load_dwordx4 a[88:91], v1, s[40:43], s46 offen offset:2048 sc1\n"
"s_cmp_lg_u32 s49, 0\n"
"s_cbranch_scc1 PS21_%=\n"
"s_mov_b64 exec, 0xffff\n"
"s_mov_b32 s67, 262144\n"
"PL19_%=:\n"
"buffer_load_dwordx2 v[44:45], v10, s[40:43], s76 offen sc1\n"
"s_waitcnt vmcnt(0)\n"
"v_cmp_ne_u64 vcc, s[56:57], v[44:45]\n"
"s_nop 3\n"
"s_cbranch_vccz PD20_%=\n"
"s_sub_u32 s67, s67, 1\n"
"s_cbranch_scc0 PL19_%=\n"
"PD20_%=:\n"
"s_mov_b64 exec, -1\n"
"PS21_%=:\n"
"s_mov_b64 exec, 0\n"
"s_cmp_eq_u32 s49, 1\n"
"s_cbranch_scc1 PAD5_%=\n"
"s_cmp_eq_u32 s49, 2\n"
"s_cbranch_scc1 PAD6_%=\n"
"s_cmp_eq_u32 s49, 3\n"
"s_cbranch_scc1 PAD7_%=\n"
"PADRET2_%=:\n"
"s_mov_b64 exec, -1\n"
"s_waitcnt lgkmcnt(0)\n"
"s_barrier\n"
"buffer_load_dwordx4 v[136:139], v1, s[40:43], s90 offen offset:0 sc1\n"
"s_nop 11\n"
"buffer_load_dwordx4 v[140:143], v1, s[40:43], s90 offen offset:1024 sc1\n"
"s_nop 11\n"
"buffer_load_dwordx4 v[144:147], v1, s[40:43], s90 offen offset:2048 sc1\n"
"s_nop 11\n"
"buffer_load_dwordx4 v[148:151], v1, s[40:43], s90 offen offset:3072 sc1\n"
"s_nop 11\n"
"s_add_u32 s90, s90, 4096\n"
"buffer_load_dwordx4 v[152:155], v1, s[40:43], s90 offen offset:0 sc1\n"
"s_nop 11\n"
"buffer_load_dwordx4 v[156:159], v1, s[40:43], s91 offen offset:0 sc1\n"
"s_nop 11\n"
"buffer_load_dwordx4 v[160:163], v1, s[40:43], s91 offen offset:1024 sc1\n"
"s_nop 11\n"
"buffer_load_dwordx4 v[164:167], v1, s[40:43], s91 offen offset:2048 sc1\n"
"s_waitcnt vmcnt(7)\n"
"v_mfma_f32_16x16x32_bf16 v[56:59], a[160:163], v[136:139], 0\n"
"buffer_load_dwordx4 v[168:171], v1, s[40:43], s91 offen offset:3072 sc1\n"
"v_mfma_f32_16x16x32_bf16 v[60:63], a[164:167], v[136:139], 0\n"
"v_mfma_f32_16x16x32_bf16 v[64:67], a[168:171], v[136:139], 0\n"
"v_mfma_f32_16x16x32_bf16 v[68:71], a[172:175], v[136:139], 0\n"
"v_mfma_f32_16x16x32_bf16 v[72:75], a[176:179], v[136:139], 0\n"
"s_waitcnt vmcnt(7)\n"
"v_mfma_f32_16x16x32_bf16 v[56:59], a[180:183], v[140:143], v[56:59]\n"
"s_add_u32 s91, s91, 4096\n"
"buffer_load_dwordx4 v[172:175], v1, s[40:43], s91 offen offset:0 sc1\n"
"v_mfma_f32_16x16x32_bf16 v[60:63], a[184:187], v[140:143], v[60:63]\n"
"v_mfma_f32_16x16x32_bf16 v[64:67], a[188:191], v[140:143], v[64:67]\n"
"v_mfma_f32_16x16x32_bf16 v[68:71], a[192:195], v[140:143], v[68:71]\n"
"v_mfma_f32_16x16x32_bf16 v[72:75], a[196:199], v[140:143], v[72:75]\n"
"s_waitcnt vmcnt(7)\n"
"v_mfma_f32_16x16x32_bf16 v[56:59], a[200:203], v[144:147], v[56:59]\n"
"buffer_load_dwordx4 v[176:179], v1, s[40:43], s92 offen offset:0 sc1\n"
"v_mfma_f32_16x16x32_bf16 v[60:63], a[204:207], v[144:147], v[60:63]\n"
"v_mfma_f32_16x16x32_bf16 v[64:67], a[208:211], v[144:147], v[64:67]\n"
"v_mfma_f32_16x16x32_bf16 v[68:71], a[212:215], v[144:147], v[68:71]\n"
"v_mfma_f32_16x16x32_bf16 v[72:75], a[216:219], v[144:147], v[72:75]\n"
"s_waitcnt vmcnt(7)\n"
"v_mfma_f32_16x16x32_bf16 v[56:59], a[220:223], v[148:151], v[56:59]\n"
"buffer_load_dwordx4 v[180:183], v1, s[40:43], s92 offen offset:1024 sc1\n"
"v_mfma_f32_16x16x32_bf16 v[60:63], a[224:227], v[148:151], v[60:63]\n"
"v_mfma_f32_16x16x32_bf16 v[64:67], a[228:231], v[148:151], v[64:67]\n"
"v_mfma_f32_16x16x32_bf16 v[68:71], a[232:235], v[148:151], v[68:71]\n"
"v_mfma_f32_16x16x32_bf16 v[72:75], a[236:239], v[148:151], v[72:75]\n"
"s_waitcnt vmcnt(7)\n"
"v_mfma_f32_16x16x32_bf16 v[56:59], a[240:243], v[152:155], v[56:59]\n"
"buffer_load_dwordx4 v[184:187], v1, s[40:43], s92 offen offset:2048 sc1\n"
"v_mfma_f32_16x16x32_bf16 v[60:63], a[244:247], v[152:155], v[60:63]\n"
"v_mfma_f32_16x16x32_bf16 v[64:67], a[248:251], v[152:155], v[64:67]\n"
"v_mfma_f32_16x16x32_bf16 v[68:71], a[252:255], v[152:155], v[68:71]\n"
"v_mfma_f32_16x16x32_bf16 v[72:75], v[252:255], v[152:155], v[72:75]\n"
"s_waitcnt vmcnt(7)\n"
"v_mfma_f32_16x16x32_bf16 v[76:79], a[160:163], v[156:159], 0\n"
"buffer_load_dwordx4 v[188:191], v1, s[40:43], s92 offen offset:3072 sc1\n"
"v_mfma_f32_16x16x32_bf16 v[80:83], a[164:167], v[156:159], 0\n"
"ds_write_b128 v12, v[56:59] offset:0\n"
"v_mfma_f32_16x16x32_bf16 v[84:87], a[168:171], v[156:159], 0\n"
"v_mfma_f32_16x16x32_bf16 v[88:91], a[172:175], v[156:159], 0\n"
"v_mfma_f32_16x16x32_bf16 v[92:95], a[176:179], v[156:159], 0\n"
"s_waitcnt vmcnt(7)\n"
"v_mfma_f32_16x16x32_bf16 v[76:79], a[180:183], v[160:163], v[76:79]\n"
"s_add_u32 s92, s92, 4096\n"
"buffer_load_dwordx4 v[192:195], v1, s[40:43], s92 offen offset:0 sc1\n"
"ds_write_b128 v12, v[60:63] offset:1024\n"
"v_mfma_f32_16x16x32_bf16 v[80:83], a[184:187], v[160:163], v[80:83]\n"
"v_mfma_f32_16x16x32_bf16 v[84:87], a[188:191], v[160:163], v[84:87]\n"
"v_mfma_f32_16x16x32_bf16 v[88:91], a[192:195], v[160:163], v[88:91]\n"
"v_mfma_f32_16x16x32_bf16 v[92:95], a[196:199], v[160:163], v[92:95]\n"
"ds_write_b128 v12, v[64:67] offset:2048\n"
"s_waitcnt vmcnt(7)\n"
"v_mfma_f32_16x16x32_bf16 v[76:79], a[200:203], v[164:167], v[76:79]\n"
"buffer_load_dwordx4 v[196:199], v1, s[40:43], s93 offen offset:0 sc1\n"
"v_mfma_f32_16x16x32_bf16 v[80:83], a[204:207], v[164:167], v[80:83]\n"
"v_mfma_f32_16x16x32_bf16 v[84:87], a[208:211], v[164:167], v[84:87]\n"
"v_mfma_f32_16x16x32_bf16 v[88:91], a[212:215], v[164:167], v[88:91]\n"
"ds_write_b128 v12, v[68:71] offset:3072\n"
"v_mfma_f32_16x16x32_bf16 v[92:95], a[216:219], v[164:167], v[92:95]\n"
"s_waitcnt vmcnt(7)\n"
"v_mfma_f32_16x16x32_bf16 v[76:79], a[220:223], v[168:171], v[76:79]\n"
"buffer_load_dwordx4 v[200:203], v1, s[40:43], s93 offen offset:1024 sc1\n"
"v_mfma_f32_16x16x32_bf16 v[80:83], a[224:227], v[168:171], v[80:83]\n"
"v_mfma_f32_16x16x32_bf16 v[84:87], a[228:231], v[168:171], v[84:87]\n"
"ds_write_b128 v12, v[72:75] offset:4096\n"
"v_mfma_f32_16x16x32_bf16 v[88:91], a[232:235], v[168:171], v[88:91]\n"
"v_mfma_f32_16x16x32_bf16 v[92:95], a[236:239], v[168:171], v[92:95]\n"
"s_waitcnt vmcnt(7)\n"
"v_mfma_f32_16x16x32_bf16 v[76:79], a[240:243], v[172:175], v[76:79]\n"
"buffer_load_dwordx4 v[204:207], v1, s[40:43], s93 offen offset:2048 sc1\n"
"v_mfma_f32_16x16x32_bf16 v[80:83], a[244:247], v[172:175], v[80:83]\n"
"v_mfma_f32_16x16x32_bf16 v[84:87], a[248:251], v[172:175], v[84:87]\n"
"v_mfma_f32_16x16x32_bf16 v[88:91], a[252:255], v[172:175], v[88:91]\n"
"v_mfma_f32_16x16x32_bf16 v[92:95], v[252:255], v[172:175], v[92:95]\n"
"s_waitcnt vmcnt(7)\n"
"v_mfma_f32_16x16x32_bf16 v[96:99], a[160:163], v[176:179], 0\n"
"buffer_load_dwordx4 v[208:211], v1, s[40:43], s93 offen offset:3072 sc1\n"
"v_mfma_f32_16x16x32_bf16 v[100:103], a[164:167], v[176:179], 0\n"
"ds_write_b128 v13, v[76:79] offset:0\n"
"v_mfma_f32_16x16x32_bf16 v[104:107], a[168:171], v[176:179], 0\n"
"v_mfma_f32_16x16x32_bf16 v[108:111], a[172:175], v[176:179], 0\n"
"ds_write_b128 v13, v[80:83] offset:1024\n"
"v_mfma_f32_16x16x32_bf16 v[112:115], a[176:179], v[176:179], 0\n"
"s_waitcnt vmcnt(7)\n"
"v_mfma_f32_16x16x32_bf16 v[96:99], a[180:183], v[180:183], v[96:99]\n"
"s_add_u32 s93, s93, 4096\n"
"buffer_load_dwordx4 v[212:215], v1, s[40:43], s93 offen offset:0 sc1\n"
"ds_write_b128 v13, v[84:87] offset:2048\n"
"v_mfma_f32_16x16x32_bf16 v[100:103], a[184:187], v[180:183], v[100:103]\n"
"v_mfma_f32_16x16x32_bf16 v[104:107], a[188:191], v[180:183], v[104:107]\n"
"ds_write_b128 v13, v[88:91] offset:3072\n"
"v_mfma_f32_16x16x32_bf16 v[108:111], a[192:195], v[180:183], v[108:111]\n"
"v_mfma_f32_16x16x32_bf16 v[112:115], a[196:199], v[180:183], v[112:115]\n"
"ds_write_b128 v13, v[92:95] offset:4096\n"
"s_waitcnt vmcnt(7)\n"
"v_mfma_f32_16x16x32_bf16 v[96:99], a[200:203], v[184:187], v[96:99]\n"
"s_add_u32 s46, s47, 61440\n"
"buffer_load_dwordx4 a[92:95], v1, s[40:43], s46 offen offset:3072 sc1\n"
"v_mfma_f32_16x16x32_bf16 v[100:103], a[204:207], v[184:187], v[100:103]\n"
"v_mfma_f32_16x16x32_bf16 v[104:107], a[208:211], v[184:187], v[104:107]\n"
"s_waitcnt lgkmcnt(0)\n"
"s_barrier\n"
"ds_read_b128 v[136:139], v15 offset:0\n"
"v_mfma_f32_16x16x32_bf16 v[108:111], a[212:215], v[184:187], v[108:111]\n"
"ds_read_b128 v[156:159], v15 offset:5120\n"
"v_mfma_f32_16x16x32_bf16 v[112:115], a[216:219], v[184:187], v[112:115]\n"
"ds_read_b128 v[140:143], v15 offset:1024\n"
"s_waitcnt vmcnt(7)\n"
"v_mfma_f32_16x16x32_bf16 v[96:99], a[220:223], v[188:191], v[96:99]\n"
"s_add_u32 s46, s21, 4096\n"
"buffer_load_dwordx4 v[216:219], v1, s[40:43], s46 offen offset:1024 sc1\n"
"ds_read_b128 v[160:163], v15 offset:6144\n"
"v_mfma_f32_16x16x32_bf16 v[100:103], a[224:227], v[188:191], v[100:103]\n"
"ds_read_b128 v[144:147], v15 offset:2048\n"
"v_mfma_f32_16x16x32_bf16 v[104:107], a[228:231], v[188:191], v[104:107]\n"
"ds_read_b128 v[164:167], v15 offset:7168\n"
"v_mfma_f32_16x16x32_bf16 v[108:111], a[232:235], v[188:191], v[108:111]\n"
"ds_read_b128 v[148:151], v15 offset:3072\n"
"v_mfma_f32_16x16x32_bf16 v[112:115], a[236:239], v[188:191], v[112:115]\n"
"ds_read_b128 v[168:171], v15 offset:8192\n"
"s_waitcnt vmcnt(7)\n"
"v_mfma_f32_16x16x32_bf16 v[96:99], a[240:243], v[192:195], v[96:99]\n"
"s_add_u32 s46, s21, 4096\n"
"buffer_load_dwordx4 v[220:223], v1, s[40:43], s46 offen offset:2048 sc1\n"
"ds_read_b128 v[152:155], v15 offset:4096\n"
"v_mfma_f32_16x16x32_bf16 v[100:103], a[244:247], v[192:195], v[100:103]\n"
"ds_read_b128 v[172:175], v15 offset:9216\n"
"v_mfma_f32_16x16x32_bf16 v[104:107], a[248:251], v[192:195], v[104:107]\n"
"s_waitcnt lgkmcnt(8)\n"
"v_add_f32 v136, v136, v156\n"
"v_add_f32 v137, v137, v157\n"
"v_add_f32 v138, v138, v158\n"
"v_add_f32 v139, v139, v159\n"
"v_mfma_f32_16x16x32_bf16 v[108:111], a[252:255], v[192:195], v[108:111]\n"
"s_waitcnt lgkmcnt(6)\n"
"v_add_f32 v140, v140, v160\n"
"v_add_f32 v141, v141, v161\n"
"v_add_f32 v142, v142, v162\n"
"v_add_f32 v143, v143, v163\n"
"v_mfma_f32_16x16x32_bf16 v[112:115], v[252:255], v[192:195], v[112:115]\n"
"s_waitcnt lgkmcnt(4)\n"
"v_add_f32 v144, v144, v164\n"
"v_add_f32 v145, v145, v165\n"
"v_add_f32 v146, v146, v166\n"
"v_add_f32 v147, v147, v167\n"
"s_waitcnt vmcnt(7)\n"
"v_mfma_f32_16x16x32_bf16 v[24:27], a[160:163], v[196:199], v[24:27]\n"
"s_add_u32 s46, s21, 4096\n"
"buffer_load_dwordx4 v[224:227], v1, s[40:43], s46 offen offset:3072 sc1\n"
"s_waitcnt lgkmcnt(2)\n"
"v_add_f32 v148, v148, v168\n"
"v_add_f32 v149, v149, v169\n"
"v_add_f32 v150, v150, v170\n"
"v_add_f32 v151, v151, v171\n"
"v_mfma_f32_16x16x32_bf16 v[28:31], a[164:167], v[196:199], v[28:31]\n"
"ds_write_b128 v14, v[96:99] offset:0\n"
"s_waitcnt lgkmcnt(1)\n"
"v_add_f32 v152, v152, v172\n"
"v_add_f32 v153, v153, v173\n"
"v_add_f32 v154, v154, v174\n"
"v_add_f32 v155, v155, v175\n"
"v_mfma_f32_16x16x32_bf16 v[32:35], a[168:171], v[196:199], v[32:35]\n"
"v_mfma_f32_16x16x32_bf16 v[36:39], a[172:175], v[196:199], v[36:39]\n"
"ds_write_b128 v14, v[100:103] offset:1024\n"
"v_mfma_f32_16x16x32_bf16 v[40:43], a[176:179], v[196:199], v[40:43]\n"
"s_waitcnt vmcnt(7)\n"
"v_mfma_f32_16x16x32_bf16 v[24:27], a[180:183], v[200:203], v[24:27]\n"
"s_add_u32 s46, s21, 8192\n"
"buffer_load_dwordx4 v[228:231], v1, s[40:43], s46 offen offset:0 sc1\n"
"ds_write_b128 v14, v[104:107] offset:2048\n"
"v_mfma_f32_16x16x32_bf16 v[28:31], a[184:187], v[200:203], v[28:31]\n"
"v_mfma_f32_16x16x32_bf16 v[32:35], a[188:191], v[200:203], v[32:35]\n"
"ds_write_b128 v14, v[108:111] offset:3072\n"
"v_mfma_f32_16x16x32_bf16 v[36:39], a[192:195], v[200:203], v[36:39]\n"
"v_mfma_f32_16x16x32_bf16 v[40:43], a[196:199], v[200:203], v[40:43]\n"
"ds_write_b128 v14, v[112:115] offset:4096\n"
"s_waitcnt vmcnt(7)\n"
"v_mfma_f32_16x16x32_bf16 v[24:27], a[200:203], v[204:207], v[24:27]\n"
"s_add_u32 s46, s21, 8192\n"
"buffer_load_dwordx4 v[232:235], v1, s[40:43], s46 offen offset:1024 sc1\n"
"v_mfma_f32_16x16x32_bf16 v[28:31], a[204:207], v[204:207], v[28:31]\n"
"v_mfma_f32_16x16x32_bf16 v[32:35], a[208:211], v[204:207], v[32:35]\n"
"s_waitcnt lgkmcnt(0)\n"
"s_barrier\n"
"ds_read_b128 v[176:179], v15 offset:10240\n"
"v_mfma_f32_16x16x32_bf16 v[36:39], a[212:215], v[204:207], v[36:39]\n"
"ds_read_b128 v[180:183], v15 offset:11264\n"
"v_mfma_f32_16x16x32_bf16 v[40:43], a[216:219], v[204:207], v[40:43]\n"
"ds_read_b128 v[184:187], v15 offset:12288\n"
"s_waitcnt vmcnt(7)\n"
"v_mfma_f32_16x16x32_bf16 v[24:27], a[220:223], v[208:211], v[24:27]\n"
"s_add_u32 s46, s21, 20480\n"
"buffer_load_dwordx4 v[236:239], v1, s[40:43], s46 offen offset:0 sc1\n"
"ds_read_b128 v[188:191], v15 offset:13312\n"
"v_mfma_f32_16x16x32_bf16 v[28:31], a[224:227], v[208:211], v[28:31]\n"
"ds_read_b128 v[192:195], v15 offset:14336\n"
"v_mfma_f32_16x16x32_bf16 v[32:35], a[228:231], v[208:211], v[32:35]\n"
"v_mfma_f32_16x16x32_bf16 v[36:39], a[232:235], v[208:211], v[36:39]\n"
"v_mfma_f32_16x16x32_bf16 v[40:43], a[236:239], v[208:211], v[40:43]\n"
"s_waitcnt lgkmcnt(4)\n"
"v_add_f32 v136, v136, v176\n"
"v_add_f32 v137, v137, v177\n"
"v_add_f32 v138, v138, v178\n"
"v_add_f32 v139, v139, v179\n"
"s_waitcnt vmcnt(7)\n"
"v_mfma_f32_16x16x32_bf16 v[24:27], a[240:243], v[212:215], v[24:27]\n"
"s_add_u32 s46, s21, 20480\n"
"buffer_load_dwordx4 v[240:243], v1, s[40:43], s46 offen offset:1024 sc1\n"
"s_waitcnt lgkmcnt(3)\n"
"v_add_f32 v140, v140, v180\n"
"v_add_f32 v141, v141, v181\n"
"v_add_f32 v142, v142, v182\n"
"v_add_f32 v143, v143, v183\n"
"v_mfma_f32_16x16x32_bf16 v[28:31], a[244:247], v[212:215], v[28:31]\n"
"s_waitcnt lgkmcnt(2)\n"
"v_add_f32 v144, v144, v184\n"
"v_add_f32 v145, v145, v185\n"
"v_add_f32 v146, v146, v186\n"
"v_add_f32 v147, v147, v187\n"
"v_mfma_f32_16x16x32_bf16 v[32:35], a[248:251], v[212:215], v[32:35]\n"
"s_waitcnt lgkmcnt(1)\n"
"v_add_f32 v148, v148, v188\n"
"v_add_f32 v149, v149, v189\n"
"v_add_f32 v150, v150, v190\n"
"v_add_f32 v151, v151, v191\n"
"v_mfma_f32_16x16x32_bf16 v[36:39], a[252:255], v[212:215], v[36:39]\n"
"s_waitcnt lgkmcnt(0)\n"
"v_add_f32 v152, v152, v192\n"
"v_add_f32 v153, v153, v193\n"
"v_add_f32 v154, v154, v194\n"
"v_add_f32 v155, v155, v195\n"
"v_mfma_f32_16x16x32_bf16 v[40:43], v[252:255], v[212:215], v[40:43]\n"
"v_pk_add_f32 v[24:25], v[24:25], v[136:137]\n"
"v_pk_add_f32 v[26:27], v[26:27], v[138:139]\n"
"v_cvt_pk_bf16_f32 v56, v24, v25\n"
"v_cvt_pk_bf16_f32 v57, v26, v27\n"
"v_pk_add_f32 v[28:29], v[28:29], v[140:141]\n"
"v_pk_add_f32 v[30:31], v[30:31], v[142:143]\n"
"v_cvt_pk_bf16_f32 v58, v28, v29\n"
"v_cvt_pk_bf16_f32 v59, v30, v31\n"
"buffer_store_dwordx4 v[56:59], v1, s[40:43], s68 offen offset:0 sc1\n"
"v_pk_add_f32 v[32:33], v[32:33], v[144:145]\n"
"v_pk_add_f32 v[34:35], v[34:35], v[146:147]\n"
"v_cvt_pk_bf16_f32 v60, v32, v33\n"
"v_cvt_pk_bf16_f32 v61, v34, v35\n"
"v_pk_add_f32 v[36:37], v[36:37], v[148:149]\n"
"v_pk_add_f32 v[38:39], v[38:39], v[150:151]\n"
"v_cvt_pk_bf16_f32 v62, v36, v37\n"
"v_cvt_pk_bf16_f32 v63, v38, v39\n"
"buffer_store_dwordx4 v[60:63], v1, s[40:43], s68 offen offset:1024 sc1\n"
"v_pk_add_f32 v[40:41], v[40:41], v[152:153]\n"
"v_pk_add_f32 v[42:43], v[42:43], v[154:155]\n"
"v_cvt_pk_bf16_f32 v64, v40, v41\n"
"v_cvt_pk_bf16_f32 v65, v42, v43\n"
"buffer_store_dwordx2 v[64:65], v1, s[40:43], s73 offen sc1\n"
"v_pk_add_f32 v[68:69], v[24:25], v[26:27]\n"
"v_pk_mul_f32 v[72:73], v[24:25], v[24:25]\n"
"v_pk_add_f32 v[70:71], v[28:29], v[30:31]\n"
"v_pk_mul_f32 v[74:75], v[26:27], v[26:27]\n"
"v_pk_fma_f32 v[72:73], v[28:29], v[28:29], v[72:73]\n"
"v_pk_fma_f32 v[74:75], v[30:31], v[30:31], v[74:75]\n"
"v_pk_add_f32 v[68:69], v[68:69], v[32:33]\n"
"v_pk_fma_f32 v[72:73], v[32:33], v[32:33], v[72:73]\n"
"v_pk_add_f32 v[70:71], v[70:71], v[34:35]\n"
"v_pk_fma_f32 v[74:75], v[34:35], v[34:35], v[74:75]\n"
"v_pk_add_f32 v[68:69], v[68:69], v[36:37]\n"
"v_pk_fma_f32 v[72:73], v[36:37], v[36:37], v[72:73]\n"
"v_pk_add_f32 v[70:71], v[70:71], v[38:39]\n"
"v_pk_fma_f32 v[74:75], v[38:39], v[38:39], v[74:75]\n"
"v_pk_add_f32 v[68:69], v[68:69], v[40:41]\n"
"v_pk_fma_f32 v[72:73], v[40:41], v[40:41], v[72:73]\n"
"v_pk_add_f32 v[70:71], v[70:71], v[42:43]\n"
"v_pk_fma_f32 v[74:75], v[42:43], v[42:43], v[74:75]\n"
"v_pk_add_f32 v[68:69], v[68:69], v[70:71]\n"
"v_pk_add_f32 v[72:73], v[72:73], v[74:75]\n"
"v_add_f32 v68, v68, v69\n"
"PAD5_%=:\n"
"s_cbranch_execz PADRET2_%=\n"
"v_add_f32 v72, v72, v73\n"
"s_nop 1\n"
"v_permlane16_swap_b32 v68, v72\n"
"v_add_f32 v68, v68, v72\n"
"v_mov_b32 v72, v68\n"
"s_nop 1\n"
"v_permlane32_swap_b32 v68, v72\n"
"v_add_f32 v68, v68, v72\n"
"s_mov_b32 exec_hi, 0\n"
"buffer_store_dword v68, v20, s[40:43], s72 offen sc1\n"
"s_mov_b32 exec_hi, -1\n"
"s_add_u32 s46, s21, 20480\n"
"buffer_load_dwordx4 v[244:247], v1, s[40:43], s46 offen offset:2048 sc1\n"
"s_lshl_b32 s76, s54, 11\n"
"s_add_u32 s76, s76, 135168\n"
"s_lshl_b32 s58, s54, 3\n"
"s_lshl_b32 s59, s49, 1\n"
"s_add_u32 s58, s58, s59\n"
"s_mul_i32 s90, s58, 20480\n"
"s_add_u32 s90, s90, 11980800\n"
"s_add_u32 s91, s90, 20480\n"
"s_lshl_b32 s96, s58, 10\n"
"s_add_u32 s96, s96, 14602240\n"
"s_lshl_b32 s97, s24, 10\n"
"s_add_u32 s97, s97, 14602240\n"
"s_and_b32 s62, s53, 7\n"
"s_mul_i32 s63, s62, 7\n"
"s_add_u32 s46, s21, 20480\n"
"buffer_load_dwordx4 v[248:251], v1, s[40:43], s46 offen offset:3072 sc1\n"
"s_min_u32 s63, s63, 48\n"
"s_and_b32 s64, s63, 1\n"
"s_add_u32 s63, s63, s64\n"
"s_lshr_b32 s63, s63, 1\n"
"s_lshr_b32 s65, s53, 3\n"
"s_mul_i32 s65, s65, 27\n"
"s_add_u32 s63, s63, s65\n"
"s_mul_i32 s65, s58, 54\n"
"s_add_u32 s63, s63, s65\n"
"s_lshl_b32 s63, s63, 10\n"
"s_add_u32 s92, s63, 14733312\n"
"s_add_u32 s93, s92, 55296\n"
"s_movk_i32 s65, 3072\n"
"s_cmp_lg_u32 s64, 0\n"
"s_cmov_b32 s65, -1016\n"
"s_add_u32 s46, s21, 0\n"
"buffer_load_dwordx4 v[196:199], v1, s[40:43], s46 offen offset:0 sc1\n"
"s_add_u32 s94, s92, s65\n"
"s_add_u32 s95, s93, s65\n"
"s_add_u32 s62, s62, 2\n"
"s_lshr_b32 s62, s62, 3\n"
"s_lshl_b32 s62, s62, 31\n"
"v_or_b32 v23, s62, v1\n"
"s_lshl_b32 s77, s54, 11\n"
"s_add_u32 s77, s77, 167936\n"
"s_lshl_b32 s62, s53, 2\n"
"s_add_u32 s62, s62, s49\n"
"s_lshl_b32 s62, s62, 3\n"
"s_add_u32 s71, s77, s62\n"
"s_lshl_b32 s62, s49, 7\n"
"s_lshl_b32 s63, s25, 4\n"
"s_add_u32 s46, s21, 0\n"
"buffer_load_dwordx4 v[200:203], v1, s[40:43], s46 offen offset:1024 sc1\n"
"s_add_u32 s62, s62, s63\n"
"v_lshrrev_b32 v22, 1, v0\n"
"v_and_b32 v21, 1, v0\n"
"v_lshlrev_b32 v21, 3, v21\n"
"v_lshl_add_u32 v22, v22, 5, v21\n"
"v_add_u32 v22, s62, v22\n"
"s_and_b32 s64, s49, 1\n"
"s_lshl_b32 s63, s64, 10\n"
"s_lshl_b32 s65, s64, 31\n"
"v_add_u32 v21, s63, v1\n"
"v_or_b32 v20, s65, v1\n"
"s_sub_u32 s58, s24, s49\n"
"s_sub_u32 s64, s22, s64\n"
"s_add_u32 s62, s49, 1\n"
"s_add_u32 s46, s21, 0\n"
"buffer_load_dwordx4 v[204:207], v1, s[40:43], s46 offen offset:2048 sc1\n"
"s_and_b32 s62, s62, 3\n"
"s_add_u32 s62, s62, s58\n"
"s_mul_i32 s62, s62, 54\n"
"s_add_u32 s62, s62, s64\n"
"s_lshl_b32 s62, s62, 10\n"
"s_add_u32 s68, s62, 14733312\n"
"s_add_u32 s62, s49, 2\n"
"s_and_b32 s62, s62, 3\n"
"s_add_u32 s62, s62, s58\n"
"s_mul_i32 s62, s62, 54\n"
"s_add_u32 s62, s62, s64\n"
"s_lshl_b32 s62, s62, 10\n"
"s_add_u32 s69, s62, 14733312\n"
"s_add_u32 s62, s49, 3\n"
"s_add_u32 s46, s21, 0\n"
"buffer_load_dwordx4 v[208:211], v1, s[40:43], s46 offen offset:3072 sc1\n"
"s_and_b32 s62, s62, 3\n"
"s_add_u32 s62, s62, s58\n"
"s_mul_i32 s62, s62, 54\n"
"s_add_u32 s62, s62, s64\n"
"s_lshl_b32 s62, s62, 10\n"
"s_add_u32 s72, s62, 14733312\n"
"s_add_u32 s62, s49, 4\n"
"s_and_b32 s62, s62, 3\n"
"s_add_u32 s62, s62, s58\n"
"s_mul_i32 s62, s62, 54\n"
"s_add_u32 s62, s62, s64\n"
"s_lshl_b32 s62, s62, 10\n"
"s_add_u32 s73, s62, 14733312\n"
"s_waitcnt vmcnt(6)\n"
"s_barrier\n"
"s_cmp_lg_u32 s49, 0\n"
"s_cbranch_scc1 FS22_%=\n"
"s_mov_b64 exec, 1\n"
"buffer_store_dwordx2 v[4:5], off, s[40:43], s70 sc1\n"
"s_mov_b64 exec, -1\n"
"FS22_%=:\n"
"s_add_u32 m0, s13, 98304\n"
"s_add_u32 s18, s7, 98304\n"
"buffer_load_dwordx4 v1, s[40:43], s18 offen offset:0 sc1 lds\n"
"buffer_load_dwordx4 v1, s[40:43], s18 offen offset:1024 sc1 lds\n"
"buffer_load_dwordx4 v1, s[40:43], s18 offen offset:2048 sc1 lds\n"
"buffer_load_dwordx4 v1, s[40:43], s18 offen offset:3072 sc1 lds\n"
"s_add_u32 m0, s13, 114688\n"
"s_add_u32 s18, s7, 114688\n"
"buffer_load_dwordx4 v1, s[40:43], s18 offen offset:0 sc1 lds\n"
"buffer_load_dwordx4 v1, s[40:43], s18 offen offset:1024 sc1 lds\n"
"buffer_load_dwordx4 v1, s[40:43], s18 offen offset:2048 sc1 lds\n"
"buffer_load_dwordx4 v1, s[40:43], s18 offen offset:3072 sc1 lds\n"
"s_add_u32 m0, s13, 131072\n"
"s_add_u32 s18, s7, 131072\n"
"buffer_load_dwordx4 v1, s[40:43], s18 offen offset:0 sc1 lds\n"
"buffer_load_dwordx4 v1, s[40:43], s18 offen offset:1024 sc1 lds\n"
"buffer_load_dwordx4 v1, s[40:43], s18 offen offset:2048 sc1 lds\n"
"buffer_load_dwordx4 v1, s[40:43], s18 offen offset:3072 sc1 lds\n"
"s_cmp_lg_u32 s49, 0\n"
"s_cbranch_scc1 PS25_%=\n"
"s_mov_b64 exec, 0xffff\n"
"s_mov_b32 s67, 262144\n"
"PL23_%=:\n"
"buffer_load_dwordx2 v[44:45], v10, s[40:43], s76 offen sc1\n"
"s_waitcnt vmcnt(0)\n"
"v_cmp_ne_u64 vcc, s[56:57], v[44:45]\n"
"s_nop 3\n"
"s_cbranch_vccz PD24_%=\n"
"s_sub_u32 s67, s67, 1\n"
"s_cbranch_scc0 PL23_%=\n"
"PD24_%=:\n"
"s_mov_b64 exec, -1\n"
"PS25_%=:\n"
"s_waitcnt vmcnt(0)\n"
"s_waitcnt lgkmcnt(0)\n"
"s_barrier\n"
"buffer_load_dwordx4 a[96:99], v1, s[40:43], s90 offen offset:0 sc1\n"
"buffer_load_dwordx4 a[100:103], v1, s[40:43], s91 offen offset:0 sc1\n"
"s_nop 9\n"
"buffer_load_dwordx4 a[104:107], v1, s[40:43], s90 offen offset:1024 sc1\n"
"buffer_load_dwordx4 a[108:111], v1, s[40:43], s91 offen offset:1024 sc1\n"
"s_nop 9\n"
"buffer_load_dwordx4 a[112:115], v1, s[40:43], s90 offen offset:2048 sc1\n"
"buffer_load_dwordx4 a[116:119], v1, s[40:43], s91 offen offset:2048 sc1\n"
"s_nop 9\n"
"buffer_load_dwordx4 a[120:123], v1, s[40:43], s90 offen offset:3072 sc1\n"
"buffer_load_dwordx4 a[124:127], v1, s[40:43], s91 offen offset:3072 sc1\n"
"s_nop 9\n"
"s_add_u32 s90, s90, 4096\n"
"buffer_load_dwordx4 a[128:131], v1, s[40:43], s90 offen offset:0 sc1\n"
"s_add_u32 s91, s91, 4096\n"
"buffer_load_dwordx4 a[132:135], v1, s[40:43], s91 offen offset:0 sc1\n"
"s_nop 9\n"
"buffer_load_dwordx4 a[136:139], v1, s[40:43], s90 offen offset:1024 sc1\n"
"buffer_load_dwordx4 a[140:143], v1, s[40:43], s91 offen offset:1024 sc1\n"
"s_nop 9\n"
"buffer_load_dwordx4 a[144:147], v1, s[40:43], s90 offen offset:2048 sc1\n"
"buffer_load_dwordx4 a[148:151], v1, s[40:43], s91 offen offset:2048 sc1\n"
"ds_read_b128 v[112:115], v1 offset:6144\n"
"ds_read_b128 v[116:119], v1 offset:0\n"
"ds_read_b128 v[120:123], v1 offset:1024\n"
"ds_read_b128 v[124:127], v1 offset:2048\n"
"ds_read_b128 v[128:131], v1 offset:3072\n"
"s_waitcnt vmcnt(13) lgkmcnt(4)\n"
"v_mfma_f32_16x16x32_bf16 v[104:107], v[112:115], a[96:99], 0\n"
"ds_read_b128 v[140:143], v1 offset:13312\n"
"s_waitcnt vmcnt(12)\n"
"v_mfma_f32_16x16x32_bf16 v[108:111], v[112:115], a[100:103], 0\n"
"ds_read_b128 v[144:147], v1 offset:7168\n"
"s_waitcnt lgkmcnt(5)\n"
"v_mfma_f32_16x16x32_bf16 v[56:59], v[116:119], a[96:99], 0\n"
"ds_read_b128 v[148:151], v1 offset:8192\n"
"v_mfma_f32_16x16x32_bf16 v[60:63], v[116:119], a[100:103], 0\n"
"ds_read_b128 v[152:155], v1 offset:9216\n"
"buffer_load_dwordx4 a[152:155], v1, s[40:43], s90 offen offset:3072 sc1\n"
"s_waitcnt lgkmcnt(6)\n"
"v_mfma_f32_16x16x32_bf16 v[64:67], v[120:123], a[96:99], 0\n"
"ds_read_b128 v[156:159], v1 offset:10240\n"
"v_mfma_f32_16x16x32_bf16 v[68:71], v[120:123], a[100:103], 0\n"
"s_waitcnt lgkmcnt(6)\n"
"v_mfma_f32_16x16x32_bf16 v[72:75], v[124:127], a[96:99], 0\n"
"v_mfma_f32_16x16x32_bf16 v[76:79], v[124:127], a[100:103], 0\n"
"buffer_load_dwordx4 a[156:159], v1, s[40:43], s91 offen offset:3072 sc1\n"
"s_waitcnt lgkmcnt(5)\n"
"v_mfma_f32_16x16x32_bf16 v[80:83], v[128:131], a[96:99], 0\n"
"v_mfma_f32_16x16x32_bf16 v[84:87], v[128:131], a[100:103], 0\n"
"s_waitcnt vmcnt(13) lgkmcnt(4)\n"
"v_mfma_f32_16x16x32_bf16 v[104:107], v[140:143], a[104:107], v[104:107]\n"
"ds_read_b128 v[112:115], v1 offset:20480\n"
"s_waitcnt vmcnt(12)\n"
"v_mfma_f32_16x16x32_bf16 v[108:111], v[140:143], a[108:111], v[108:111]\n"
"ds_read_b128 v[116:119], v1 offset:14336\n"
"s_waitcnt lgkmcnt(5)\n"
"v_mfma_f32_16x16x32_bf16 v[56:59], v[144:147], a[104:107], v[56:59]\n"
"ds_read_b128 v[120:123], v1 offset:15360\n"
"v_mfma_f32_16x16x32_bf16 v[60:63], v[144:147], a[108:111], v[60:63]\n"
"ds_read_b128 v[124:127], v1 offset:16384\n"
"s_add_u32 s90, s90, 4096\n"
"buffer_load_dwordx4 a[160:163], v1, s[40:43], s90 offen offset:0 sc1\n"
"s_waitcnt lgkmcnt(6)\n"
"v_mfma_f32_16x16x32_bf16 v[64:67], v[148:151], a[104:107], v[64:67]\n"
"ds_read_b128 v[128:131], v1 offset:17408\n"
"v_mfma_f32_16x16x32_bf16 v[68:71], v[148:151], a[108:111], v[68:71]\n"
"s_waitcnt lgkmcnt(6)\n"
"v_mfma_f32_16x16x32_bf16 v[72:75], v[152:155], a[104:107], v[72:75]\n"
"v_mfma_f32_16x16x32_bf16 v[76:79], v[152:155], a[108:111], v[76:79]\n"
"s_add_u32 s91, s91, 4096\n"
"buffer_load_dwordx4 a[164:167], v1, s[40:43], s91 offen offset:0 sc1\n"
"s_waitcnt lgkmcnt(5)\n"
"v_mfma_f32_16x16x32_bf16 v[80:83], v[156:159], a[104:107], v[80:83]\n"
"v_mfma_f32_16x16x32_bf16 v[84:87], v[156:159], a[108:111], v[84:87]\n"
"s_waitcnt vmcnt(13) lgkmcnt(4)\n"
"v_mfma_f32_16x16x32_bf16 v[104:107], v[112:115], a[112:115], v[104:107]\n"
"ds_read_b128 v[140:143], v1 offset:27648\n"
"s_waitcnt vmcnt(12)\n"
"v_mfma_f32_16x16x32_bf16 v[108:111], v[112:115], a[116:119], v[108:111]\n"
"ds_read_b128 v[144:147], v1 offset:21504\n"
"s_waitcnt lgkmcnt(5)\n"
"v_mfma_f32_16x16x32_bf16 v[56:59], v[116:119], a[112:115], v[56:59]\n"
"ds_read_b128 v[148:151], v1 offset:22528\n"
"v_mfma_f32_16x16x32_bf16 v[60:63], v[116:119], a[116:119], v[60:63]\n"
"ds_read_b128 v[152:155], v1 offset:23552\n"
"buffer_load_dwordx4 a[168:171], v1, s[40:43], s90 offen offset:1024 sc1\n"
"s_waitcnt lgkmcnt(6)\n"
"v_mfma_f32_16x16x32_bf16 v[64:67], v[120:123], a[112:115], v[64:67]\n"
"ds_read_b128 v[156:159], v1 offset:24576\n"
"v_mfma_f32_16x16x32_bf16 v[68:71], v[120:123], a[116:119], v[68:71]\n"
"s_waitcnt lgkmcnt(6)\n"
"v_mfma_f32_16x16x32_bf16 v[72:75], v[124:127], a[112:115], v[72:75]\n"
"v_mfma_f32_16x16x32_bf16 v[76:79], v[124:127], a[116:119], v[76:79]\n"
"buffer_load_dwordx4 a[172:175], v1, s[40:43], s91 offen offset:1024 sc1\n"
"s_waitcnt lgkmcnt(5)\n"
"v_mfma_f32_16x16x32_bf16 v[80:83], v[128:131], a[112:115], v[80:83]\n"
"v_mfma_f32_16x16x32_bf16 v[84:87], v[128:131], a[116:119], v[84:87]\n"
"s_waitcnt vmcnt(13) lgkmcnt(4)\n"
"v_mfma_f32_16x16x32_bf16 v[104:107], v[140:143], a[120:123], v[104:107]\n"
"ds_read_b128 v[112:115], v1 offset:34816\n"
"s_waitcnt vmcnt(12)\n"
"v_mfma_f32_16x16x32_bf16 v[108:111], v[140:143], a[124:127], v[108:111]\n"
"ds_read_b128 v[116:119], v1 offset:28672\n"
"s_waitcnt lgkmcnt(5)\n"
"v_mfma_f32_16x16x32_bf16 v[56:59], v[144:147], a[120:123], v[56:59]\n"
"ds_read_b128 v[120:123], v1 offset:29696\n"
"v_mfma_f32_16x16x32_bf16 v[60:63], v[144:147], a[124:127], v[60:63]\n"
"ds_read_b128 v[124:127], v1 offset:30720\n"
"buffer_load_dwordx4 v[132:135], v1, s[40:43], s96 offen offset:0 sc1\n"
"s_waitcnt lgkmcnt(6)\n"
"v_mfma_f32_16x16x32_bf16 v[64:67], v[148:151], a[120:123], v[64:67]\n"
"ds_read_b128 v[128:131], v1 offset:31744\n"
"v_mfma_f32_16x16x32_bf16 v[68:71], v[148:151], a[124:127], v[68:71]\n"
"s_waitcnt lgkmcnt(6)\n"
"v_mfma_f32_16x16x32_bf16 v[72:75], v[152:155], a[120:123], v[72:75]\n"
"v_mfma_f32_16x16x32_bf16 v[76:79], v[152:155], a[124:127], v[76:79]\n"
"buffer_load_dwordx4 v[160:163], v1, s[40:43], s96 offen offset:1024 sc1\n"
"s_waitcnt lgkmcnt(5)\n"
"v_mfma_f32_16x16x32_bf16 v[80:83], v[156:159], a[120:123], v[80:83]\n"
"v_mfma_f32_16x16x32_bf16 v[84:87], v[156:159], a[124:127], v[84:87]\n"
"s_waitcnt vmcnt(13) lgkmcnt(4)\n"
"v_mfma_f32_16x16x32_bf16 v[104:107], v[112:115], a[128:131], v[104:107]\n"
"ds_read_b128 v[140:143], v1 offset:41984\n"
"s_waitcnt vmcnt(12)\n"
"v_mfma_f32_16x16x32_bf16 v[108:111], v[112:115], a[132:135], v[108:111]\n"
"ds_read_b128 v[144:147], v1 offset:35840\n"
"s_waitcnt lgkmcnt(5)\n"
"v_mfma_f32_16x16x32_bf16 v[56:59], v[116:119], a[128:131], v[56:59]\n"
"ds_read_b128 v[148:151], v1 offset:36864\n"
"v_mfma_f32_16x16x32_bf16 v[60:63], v[116:119], a[132:135], v[60:63]\n"
"ds_read_b128 v[152:155], v1 offset:37888\n"
"buffer_load_dwordx4 v[52:55], v1, s[40:43], s97 offen sc1\n"
"s_waitcnt lgkmcnt(6)\n"
"v_mfma_f32_16x16x32_bf16 v[64:67], v[120:123], a[128:131], v[64:67]\n"
"ds_read_b128 v[156:159], v1 offset:38912\n"
"v_mfma_f32_16x16x32_bf16 v[68:71], v[120:123], a[132:135], v[68:71]\n"
"s_waitcnt lgkmcnt(6)\n"
"v_mfma_f32_16x16x32_bf16 v[72:75], v[124:127], a[128:131], v[72:75]\n"
"v_mfma_f32_16x16x32_bf16 v[76:79], v[124:127], a[132:135], v[76:79]\n"
"buffer_load_dwordx4 a[176:179], v1, s[40:43], s90 offen offset:2048 sc1\n"
"s_waitcnt lgkmcnt(5)\n"
"v_mfma_f32_16x16x32_bf16 v[80:83], v[128:131], a[128:131], v[80:83]\n"
"v_mfma_f32_16x16x32_bf16 v[84:87], v[128:131], a[132:135], v[84:87]\n"
"s_waitcnt vmcnt(13) lgkmcnt(4)\n"
"v_mfma_f32_16x16x32_bf16 v[104:107], v[140:143], a[136:139], v[104:107]\n"
"ds_read_b128 v[112:115], v1 offset:49152\n"
"s_waitcnt vmcnt(12)\n"
"v_mfma_f32_16x16x32_bf16 v[108:111], v[140:143], a[140:143], v[108:111]\n"
"ds_read_b128 v[116:119], v1 offset:43008\n"
"s_waitcnt lgkmcnt(5)\n"
"v_mfma_f32_16x16x32_bf16 v[56:59], v[144:147], a[136:139], v[56:59]\n"
"ds_read_b128 v[120:123], v1 offset:44032\n"
"v_mfma_f32_16x16x32_bf16 v[60:63], v[144:147], a[140:143], v[60:63]\n"
"ds_read_b128 v[124:127], v1 offset:45056\n"
"buffer_load_dwordx4 a[180:183], v1, s[40:43], s91 offen offset:2048 sc1\n"
"s_waitcnt lgkmcnt(6)\n"
"v_mfma_f32_16x16x32_bf16 v[64:67], v[148:151], a[136:139], v[64:67]\n"
"ds_read_b128 v[128:131], v1 offset:46080\n"
"v_mfma_f32_16x16x32_bf16 v[68:71], v[148:151], a[140:143], v[68:71]\n"
"s_waitcnt lgkmcnt(6)\n"
"v_mfma_f32_16x16x32_bf16 v[72:75], v[152:155], a[136:139], v[72:75]\n"
"v_mfma_f32_16x16x32_bf16 v[76:79], v[152:155], a[140:143], v[76:79]\n"
"buffer_load_dwordx4 a[184:187], v1, s[40:43], s90 offen offset:3072 sc1\n"
"s_waitcnt lgkmcnt(5)\n"
"v_mfma_f32_16x16x32_bf16 v[80:83], v[156:159], a[136:139], v[80:83]\n"
"v_mfma_f32_16x16x32_bf16 v[84:87], v[156:159], a[140:143], v[84:87]\n"
"s_waitcnt vmcnt(13) lgkmcnt(4)\n"
"v_mfma_f32_16x16x32_bf16 v[104:107], v[112:115], a[144:147], v[104:107]\n"
"ds_read_b128 v[140:143], v1 offset:56320\n"
"s_waitcnt vmcnt(12)\n"
"v_mfma_f32_16x16x32_bf16 v[108:111], v[112:115], a[148:151], v[108:111]\n"
"ds_read_b128 v[144:147], v1 offset:50176\n"
"s_waitcnt lgkmcnt(5)\n"
"v_mfma_f32_16x16x32_bf16 v[56:59], v[116:119], a[144:147], v[56:59]\n"
"ds_read_b128 v[148:151], v1 offset:51200\n"
"v_mfma_f32_16x16x32_bf16 v[60:63], v[116:119], a[148:151], v[60:63]\n"
"ds_read_b128 v[152:155], v1 offset:52224\n"
"buffer_load_dwordx4 a[188:191], v1, s[40:43], s91 offen offset:3072 sc1\n"
"s_waitcnt lgkmcnt(6)\n"
"v_mfma_f32_16x16x32_bf16 v[64:67], v[120:123], a[144:147], v[64:67]\n"
"ds_read_b128 v[156:159], v1 offset:53248\n"
"v_mfma_f32_16x16x32_bf16 v[68:71], v[120:123], a[148:151], v[68:71]\n"
"s_waitcnt lgkmcnt(6)\n"
"v_mfma_f32_16x16x32_bf16 v[72:75], v[124:127], a[144:147], v[72:75]\n"
"v_mfma_f32_16x16x32_bf16 v[76:79], v[124:127], a[148:151], v[76:79]\n"
"s_add_u32 s90, s90, 4096\n"
"buffer_load_dwordx4 a[192:195], v1, s[40:43], s90 offen offset:0 sc1\n"
"s_waitcnt lgkmcnt(5)\n"
"v_mfma_f32_16x16x32_bf16 v[80:83], v[128:131], a[144:147], v[80:83]\n"
"v_mfma_f32_16x16x32_bf16 v[84:87], v[128:131], a[148:151], v[84:87]\n"
"s_waitcnt vmcnt(13) lgkmcnt(4)\n"
"v_mfma_f32_16x16x32_bf16 v[104:107], v[140:143], a[152:155], v[104:107]\n"
"ds_read_b128 v[112:115], v1 offset:63488\n"
"s_waitcnt vmcnt(12)\n"
"v_mfma_f32_16x16x32_bf16 v[108:111], v[140:143], a[156:159], v[108:111]\n"
"ds_read_b128 v[116:119], v1 offset:57344\n"
"s_waitcnt lgkmcnt(5)\n"
"v_mfma_f32_16x16x32_bf16 v[56:59], v[144:147], a[152:155], v[56:59]\n"
"ds_read_b128 v[120:123], v1 offset:58368\n"
"v_mfma_f32_16x16x32_bf16 v[60:63], v[144:147], a[156:159], v[60:63]\n"
"ds_read_b128 v[124:127], v1 offset:59392\n"
"s_add_u32 s91, s91, 4096\n"
"buffer_load_dwordx4 a[196:199], v1, s[40:43], s91 offen offset:0 sc1\n"
"s_waitcnt lgkmcnt(6)\n"
"v_mfma_f32_16x16x32_bf16 v[64:67], v[148:151], a[152:155], v[64:67]\n"
"ds_read_b128 v[128:131], v1 offset:60416\n"
"v_mfma_f32_16x16x32_bf16 v[68:71], v[148:151], a[156:159], v[68:71]\n"
"s_waitcnt lgkmcnt(6)\n"
"v_mfma_f32_16x16x32_bf16 v[72:75], v[152:155], a[152:155], v[72:75]\n"
"v_mfma_f32_16x16x32_bf16 v[76:79], v[152:155], a[156:159], v[76:79]\n"
"buffer_load_dwordx4 a[200:203], v1, s[40:43], s90 offen offset:1024 sc1\n"
"s_waitcnt lgkmcnt(5)\n"
"v_mfma_f32_16x16x32_bf16 v[80:83], v[156:159], a[152:155], v[80:83]\n"
"v_mfma_f32_16x16x32_bf16 v[84:87], v[156:159], a[156:159], v[84:87]\n"
"s_waitcnt vmcnt(13) lgkmcnt(4)\n"
"v_mfma_f32_16x16x32_bf16 v[104:107], v[112:115], a[160:163], v[104:107]\n"
"ds_read_b128 v[140:143], v2 offset:5120\n"
"s_waitcnt vmcnt(12)\n"
"v_mfma_f32_16x16x32_bf16 v[108:111], v[112:115], a[164:167], v[108:111]\n"
"ds_read_b128 v[144:147], v1 offset:64512\n"
"s_waitcnt lgkmcnt(5)\n"
"v_mfma_f32_16x16x32_bf16 v[56:59], v[116:119], a[160:163], v[56:59]\n"
"ds_read_b128 v[148:151], v2 offset:0\n"
"v_mfma_f32_16x16x32_bf16 v[60:63], v[116:119], a[164:167], v[60:63]\n"
"ds_read_b128 v[152:155], v2 offset:1024\n"
"buffer_load_dwordx4 a[204:207], v1, s[40:43], s91 offen offset:1024 sc1\n"
"s_waitcnt lgkmcnt(6)\n"
"v_mfma_f32_16x16x32_bf16 v[64:67], v[120:123], a[160:163], v[64:67]\n"
"ds_read_b128 v[156:159], v2 offset:2048\n"
"v_mfma_f32_16x16x32_bf16 v[68:71], v[120:123], a[164:167], v[68:71]\n"
"s_waitcnt lgkmcnt(6)\n"
"v_mfma_f32_16x16x32_bf16 v[72:75], v[124:127], a[160:163], v[72:75]\n"
"v_mfma_f32_16x16x32_bf16 v[76:79], v[124:127], a[164:167], v[76:79]\n"
"buffer_load_dwordx4 a[208:211], v1, s[40:43], s90 offen offset:2048 sc1\n"
"s_waitcnt lgkmcnt(5)\n"
"v_mfma_f32_16x16x32_bf16 v[80:83], v[128:131], a[160:163], v[80:83]\n"
"v_mfma_f32_16x16x32_bf16 v[84:87], v[128:131], a[164:167], v[84:87]\n"
"s_waitcnt vmcnt(13) lgkmcnt(4)\n"
"v_mfma_f32_16x16x32_bf16 v[104:107], v[140:143], a[168:171], v[104:107]\n"
"ds_read_b128 v[112:115], v2 offset:12288\n"
"s_waitcnt vmcnt(12)\n"
"v_mfma_f32_16x16x32_bf16 v[108:111], v[140:143], a[172:175], v[108:111]\n"
"ds_read_b128 v[116:119], v2 offset:6144\n"
"s_waitcnt lgkmcnt(5)\n"
"v_mfma_f32_16x16x32_bf16 v[56:59], v[144:147], a[168:171], v[56:59]\n"
"ds_read_b128 v[120:123], v2 offset:7168\n"
"v_mfma_f32_16x16x32_bf16 v[60:63], v[144:147], a[172:175], v[60:63]\n"
"ds_read_b128 v[124:127], v2 offset:8192\n"
"buffer_load_dwordx4 a[212:215], v1, s[40:43], s91 offen offset:2048 sc1\n"
"s_waitcnt lgkmcnt(6)\n"
"v_mfma_f32_16x16x32_bf16 v[64:67], v[148:151], a[168:171], v[64:67]\n"
"ds_read_b128 v[128:131], v2 offset:9216\n"
"v_mfma_f32_16x16x32_bf16 v[68:71], v[148:151], a[172:175], v[68:71]\n"
"s_waitcnt vmcnt(12)\n"
"v_add_f32 v132, v132, v134\n"
"s_waitcnt lgkmcnt(6)\n"
"v_mfma_f32_16x16x32_bf16 v[72:75], v[152:155], a[168:171], v[72:75]\n"
"s_waitcnt vmcnt(11)\n"
"v_add_f32 v160, v160, v162\n"
"v_mfma_f32_16x16x32_bf16 v[76:79], v[152:155], a[172:175], v[76:79]\n"
"buffer_load_dwordx4 a[216:219], v1, s[40:43], s90 offen offset:3072 sc1\n"
"s_waitcnt lgkmcnt(5)\n"
"v_mfma_f32_16x16x32_bf16 v[80:83], v[156:159], a[168:171], v[80:83]\n"
"s_waitcnt vmcnt(11)\n"
"v_pk_add_f32 v[52:53], v[52:53], v[54:55]\n"
"v_mfma_f32_16x16x32_bf16 v[84:87], v[156:159], a[172:175], v[84:87]\n"
"v_permlane16_swap_b32 v132, v160\n"
"s_waitcnt vmcnt(10) lgkmcnt(4)\n"
"v_mfma_f32_16x16x32_bf16 v[104:107], v[112:115], a[176:179], v[104:107]\n"
"ds_read_b128 v[140:143], v2 offset:19456\n"
"s_waitcnt vmcnt(9)\n"
"v_mfma_f32_16x16x32_bf16 v[108:111], v[112:115], a[180:183], v[108:111]\n"
"ds_read_b128 v[144:147], v2 offset:13312\n"
"s_waitcnt lgkmcnt(5)\n"
"v_mfma_f32_16x16x32_bf16 v[56:59], v[116:119], a[176:179], v[56:59]\n"
"ds_read_b128 v[148:151], v2 offset:14336\n"
"v_mfma_f32_16x16x32_bf16 v[60:63], v[116:119], a[180:183], v[60:63]\n"
"ds_read_b128 v[152:155], v2 offset:15360\n"
"buffer_load_dwordx4 a[220:223], v1, s[40:43], s91 offen offset:3072 sc1\n"
"s_waitcnt lgkmcnt(6)\n"
"v_mfma_f32_16x16x32_bf16 v[64:67], v[120:123], a[176:179], v[64:67]\n"
"ds_read_b128 v[156:159], v2 offset:16384\n"
"v_mfma_f32_16x16x32_bf16 v[68:71], v[120:123], a[180:183], v[68:71]\n"
"v_permlane16_swap_b32 v52, v53\n"
"s_waitcnt lgkmcnt(6)\n"
"v_mfma_f32_16x16x32_bf16 v[72:75], v[124:127], a[176:179], v[72:75]\n"
"v_add_f32 v132, v132, v160\n"
"v_mfma_f32_16x16x32_bf16 v[76:79], v[124:127], a[180:183], v[76:79]\n"
"s_add_u32 s90, s90, 4096\n"
"buffer_load_dwordx4 a[224:227], v1, s[40:43], s90 offen offset:0 sc1\n"
"s_waitcnt lgkmcnt(5)\n"
"v_mfma_f32_16x16x32_bf16 v[80:83], v[128:131], a[176:179], v[80:83]\n"
"v_add_f32 v52, v52, v53\n"
"v_mfma_f32_16x16x32_bf16 v[84:87], v[128:131], a[180:183], v[84:87]\n"
"v_mov_b32 v160, v132\n"
"s_waitcnt vmcnt(10) lgkmcnt(4)\n"
"v_mfma_f32_16x16x32_bf16 v[104:107], v[140:143], a[184:187], v[104:107]\n"
"ds_read_b128 v[112:115], v2 offset:26624\n"
"s_waitcnt vmcnt(9)\n"
"v_mfma_f32_16x16x32_bf16 v[108:111], v[140:143], a[188:191], v[108:111]\n"
"ds_read_b128 v[116:119], v2 offset:20480\n"
"s_waitcnt lgkmcnt(5)\n"
"v_mfma_f32_16x16x32_bf16 v[56:59], v[144:147], a[184:187], v[56:59]\n"
"ds_read_b128 v[120:123], v2 offset:21504\n"
"v_mfma_f32_16x16x32_bf16 v[60:63], v[144:147], a[188:191], v[60:63]\n"
"ds_read_b128 v[124:127], v2 offset:22528\n"
"s_add_u32 s91, s91, 4096\n"
"buffer_load_dwordx4 a[228:231], v1, s[40:43], s91 offen offset:0 sc1\n"
"s_waitcnt lgkmcnt(6)\n"
"v_mfma_f32_16x16x32_bf16 v[64:67], v[148:151], a[184:187], v[64:67]\n"
"ds_read_b128 v[128:131], v2 offset:23552\n"
"v_mfma_f32_16x16x32_bf16 v[68:71], v[148:151], a[188:191], v[68:71]\n"
"v_mov_b32 v53, v52\n"
"s_waitcnt lgkmcnt(6)\n"
"v_mfma_f32_16x16x32_bf16 v[72:75], v[152:155], a[184:187], v[72:75]\n"
"v_permlane32_swap_b32 v132, v160\n"
"v_mfma_f32_16x16x32_bf16 v[76:79], v[152:155], a[188:191], v[76:79]\n"
"buffer_load_dwordx4 a[232:235], v1, s[40:43], s90 offen offset:1024 sc1\n"
"s_waitcnt lgkmcnt(5)\n"
"v_mfma_f32_16x16x32_bf16 v[80:83], v[156:159], a[184:187], v[80:83]\n"
"v_permlane32_swap_b32 v52, v53\n"
"v_mfma_f32_16x16x32_bf16 v[84:87], v[156:159], a[188:191], v[84:87]\n"
"v_add_f32 v132, v132, v160\n"
"s_waitcnt vmcnt(10) lgkmcnt(4)\n"
"v_mfma_f32_16x16x32_bf16 v[104:107], v[112:115], a[192:195], v[104:107]\n"
"ds_read_b128 v[140:143], v2 offset:33792\n"
"v_add_f32 v52, v52, v53\n"
"s_waitcnt vmcnt(9)\n"
"v_mfma_f32_16x16x32_bf16 v[108:111], v[112:115], a[196:199], v[108:111]\n"
"ds_read_b128 v[144:147], v2 offset:27648\n"
"v_mul_f32 v16, -0.0015625, v132\n"
"s_waitcnt lgkmcnt(5)\n"
"v_mfma_f32_16x16x32_bf16 v[56:59], v[116:119], a[192:195], v[56:59]\n"
"ds_read_b128 v[148:151], v2 offset:28672\n"
"v_mul_f32 v52, 0.0015625, v52\n"
"v_mfma_f32_16x16x32_bf16 v[60:63], v[116:119], a[196:199], v[60:63]\n"
"ds_read_b128 v[152:155], v2 offset:29696\n"
"buffer_load_dwordx4 a[236:239], v1, s[40:43], s91 offen offset:1024 sc1\n"
"s_waitcnt lgkmcnt(6)\n"
"v_mfma_f32_16x16x32_bf16 v[64:67], v[120:123], a[192:195], v[64:67]\n"
"ds_read_b128 v[156:159], v2 offset:30720\n"
"v_mov_b32 v17, v16\n"
"v_mfma_f32_16x16x32_bf16 v[68:71], v[120:123], a[196:199], v[68:71]\n"
"v_mov_b32 v53, v52\n"
"s_waitcnt lgkmcnt(6)\n"
"v_mfma_f32_16x16x32_bf16 v[72:75], v[124:127], a[192:195], v[72:75]\n"
"v_permlane16_swap_b32 v16, v17\n"
"v_mfma_f32_16x16x32_bf16 v[76:79], v[124:127], a[196:199], v[76:79]\n"
"buffer_load_dwordx4 a[240:243], v1, s[40:43], s90 offen offset:2048 sc1\n"
"s_waitcnt lgkmcnt(5)\n"
"v_mfma_f32_16x16x32_bf16 v[80:83], v[128:131], a[192:195], v[80:83]\n"
"v_permlane16_swap_b32 v52, v53\n"
"v_mfma_f32_16x16x32_bf16 v[84:87], v[128:131], a[196:199], v[84:87]\n"
"v_fma_f32 v53, -v52, v52, v53\n"
"s_waitcnt vmcnt(10) lgkmcnt(4)\n"
"v_mfma_f32_16x16x32_bf16 v[104:107], v[140:143], a[200:203], v[104:107]\n"
"ds_read_b128 v[112:115], v2 offset:40960\n"
"v_max_f32 v53, 0, v53\n"
"s_waitcnt vmcnt(9)\n"
"v_mfma_f32_16x16x32_bf16 v[108:111], v[140:143], a[204:207], v[108:111]\n"
"ds_read_b128 v[116:119], v2 offset:34816\n"
"v_add_f32 v53, 1e-05, v53\n"
"s_waitcnt lgkmcnt(5)\n"
"v_mfma_f32_16x16x32_bf16 v[56:59], v[144:147], a[200:203], v[56:59]\n"
"ds_read_b128 v[120:123], v2 offset:35840\n"
"v_rsq_f32 v18, v53\n"
"v_mfma_f32_16x16x32_bf16 v[60:63], v[144:147], a[204:207], v[60:63]\n"
"ds_read_b128 v[124:127], v2 offset:36864\n"
"buffer_load_dwordx4 a[244:247], v1, s[40:43], s91 offen offset:2048 sc1\n"
"s_waitcnt lgkmcnt(6)\n"
"v_mfma_f32_16x16x32_bf16 v[64:67], v[148:151], a[200:203], v[64:67]\n"
"ds_read_b128 v[128:131], v2 offset:37888\n"
"ds_read_b128 v[168:171], v19 offset:0\n"
"v_mfma_f32_16x16x32_bf16 v[68:71], v[148:151], a[204:207], v[68:71]\n"
"ds_read_b128 v[172:175], v19 offset:64\n"
"s_waitcnt lgkmcnt(8)\n"
"v_mfma_f32_16x16x32_bf16 v[72:75], v[152:155], a[200:203], v[72:75]\n"
"ds_read_b128 v[176:179], v19 offset:128\n"
"v_mfma_f32_16x16x32_bf16 v[76:79], v[152:155], a[204:207], v[76:79]\n"
"buffer_load_dwordx4 a[248:251], v1, s[40:43], s90 offen offset:3072 sc1\n"
"s_waitcnt lgkmcnt(8)\n"
"v_mfma_f32_16x16x32_bf16 v[80:83], v[156:159], a[200:203], v[80:83]\n"
"ds_read_b128 v[180:183], v19 offset:192\n"
"v_mfma_f32_16x16x32_bf16 v[84:87], v[156:159], a[204:207], v[84:87]\n"
"ds_read_b128 v[184:187], v19 offset:256\n"
"s_waitcnt vmcnt(10) lgkmcnt(9)\n"
"v_mfma_f32_16x16x32_bf16 v[104:107], v[112:115], a[208:211], v[104:107]\n"
"ds_read_b128 v[140:143], v2 offset:48128\n"
"ds_read_b128 v[188:191], v19 offset:320\n"
"s_waitcnt vmcnt(9)\n"
"v_mfma_f32_16x16x32_bf16 v[108:111], v[112:115], a[212:215], v[108:111]\n"
"ds_read_b128 v[144:147], v2 offset:41984\n"
"ds_read_b128 v[192:195], v19 offset:384\n"
"s_waitcnt lgkmcnt(12)\n"
"v_mfma_f32_16x16x32_bf16 v[56:59], v[116:119], a[208:211], v[56:59]\n"
"ds_read_b128 v[148:151], v2 offset:43008\n"
"v_mfma_f32_16x16x32_bf16 v[60:63], v[116:119], a[212:215], v[60:63]\n"
"ds_read_b128 v[152:155], v2 offset:44032\n"
"buffer_load_dwordx4 a[252:255], v1, s[40:43], s91 offen offset:3072 sc1\n"
"s_waitcnt lgkmcnt(13)\n"
"v_mfma_f32_16x16x32_bf16 v[64:67], v[120:123], a[208:211], v[64:67]\n"
"ds_read_b128 v[156:159], v2 offset:45056\n"
"v_mfma_f32_16x16x32_bf16 v[68:71], v[120:123], a[212:215], v[68:71]\n"
"s_waitcnt lgkmcnt(13)\n"
"v_mfma_f32_16x16x32_bf16 v[72:75], v[124:127], a[208:211], v[72:75]\n"
"v_mfma_f32_16x16x32_bf16 v[76:79], v[124:127], a[212:215], v[76:79]\n"
"s_add_u32 s46, s21, 4096\n"
"buffer_load_dwordx4 v[212:215], v1, s[40:43], s46 offen offset:0 sc1\n"
"s_waitcnt lgkmcnt(12)\n"
"v_mfma_f32_16x16x32_bf16 v[80:83], v[128:131], a[208:211], v[80:83]\n"
"v_mfma_f32_16x16x32_bf16 v[84:87], v[128:131], a[212:215], v[84:87]\n"
"s_waitcnt vmcnt(10) lgkmcnt(6)\n"
"v_mfma_f32_16x16x32_bf16 v[104:107], v[140:143], a[216:219], v[104:107]\n"
"ds_read_b128 v[112:115], v2 offset:55296\n"
"s_waitcnt vmcnt(9)\n"
"v_mfma_f32_16x16x32_bf16 v[108:111], v[140:143], a[220:223], v[108:111]\n"
"ds_read_b128 v[116:119], v2 offset:49152\n"
"s_waitcnt lgkmcnt(6)\n"
"v_mfma_f32_16x16x32_bf16 v[56:59], v[144:147], a[216:219], v[56:59]\n"
"ds_read_b128 v[120:123], v2 offset:50176\n"
"v_mfma_f32_16x16x32_bf16 v[60:63], v[144:147], a[220:223], v[60:63]\n"
"ds_read_b128 v[124:127], v2 offset:51200\n"
"s_add_u32 s46, s21, 24576\n"
"buffer_load_dwordx4 v[252:255], v1, s[40:43], s46 offen offset:0 sc1\n"
"s_waitcnt lgkmcnt(6)\n"
"v_mfma_f32_16x16x32_bf16 v[64:67], v[148:151], a[216:219], v[64:67]\n"
"ds_read_b128 v[128:131], v2 offset:52224\n"
"v_mfma_f32_16x16x32_bf16 v[68:71], v[148:151], a[220:223], v[68:71]\n"
"s_waitcnt lgkmcnt(6)\n"
"v_mfma_f32_16x16x32_bf16 v[72:75], v[152:155], a[216:219], v[72:75]\n"
"v_mfma_f32_16x16x32_bf16 v[76:79], v[152:155], a[220:223], v[76:79]\n"
"s_waitcnt lgkmcnt(5)\n"
"v_mfma_f32_16x16x32_bf16 v[80:83], v[156:159], a[216:219], v[80:83]\n"
"v_mfma_f32_16x16x32_bf16 v[84:87], v[156:159], a[220:223], v[84:87]\n"
"s_waitcnt vmcnt(9) lgkmcnt(4)\n"
"v_mfma_f32_16x16x32_bf16 v[104:107], v[112:115], a[224:227], v[104:107]\n"
"ds_read_b128 v[140:143], v2 offset:62464\n"
"s_waitcnt vmcnt(8)\n"
"v_mfma_f32_16x16x32_bf16 v[108:111], v[112:115], a[228:231], v[108:111]\n"
"ds_read_b128 v[144:147], v2 offset:56320\n"
"s_waitcnt lgkmcnt(5)\n"
"v_mfma_f32_16x16x32_bf16 v[56:59], v[116:119], a[224:227], v[56:59]\n"
"ds_read_b128 v[148:151], v2 offset:57344\n"
"v_mfma_f32_16x16x32_bf16 v[60:63], v[116:119], a[228:231], v[60:63]\n"
"ds_read_b128 v[152:155], v2 offset:58368\n"
"s_waitcnt lgkmcnt(6)\n"
"v_mfma_f32_16x16x32_bf16 v[64:67], v[120:123], a[224:227], v[64:67]\n"
"ds_read_b128 v[156:159], v2 offset:59392\n"
"v_mfma_f32_16x16x32_bf16 v[68:71], v[120:123], a[228:231], v[68:71]\n"
"s_waitcnt lgkmcnt(6)\n"
"v_mfma_f32_16x16x32_bf16 v[72:75], v[124:127], a[224:227], v[72:75]\n"
"v_mfma_f32_16x16x32_bf16 v[76:79], v[124:127], a[228:231], v[76:79]\n"
"s_waitcnt lgkmcnt(5)\n"
"v_mfma_f32_16x16x32_bf16 v[80:83], v[128:131], a[224:227], v[80:83]\n"
"v_mfma_f32_16x16x32_bf16 v[84:87], v[128:131], a[228:231], v[84:87]\n"
"s_waitcnt vmcnt(7) lgkmcnt(4)\n"
"v_mfma_f32_16x16x32_bf16 v[104:107], v[140:143], a[232:235], v[104:107]\n"
"ds_read_b128 v[112:115], v3 offset:4096\n"
"s_waitcnt vmcnt(6)\n"
"v_mfma_f32_16x16x32_bf16 v[108:111], v[140:143], a[236:239], v[108:111]\n"
"ds_read_b128 v[116:119], v2 offset:63488\n"
"s_waitcnt lgkmcnt(5)\n"
"v_mfma_f32_16x16x32_bf16 v[56:59], v[144:147], a[232:235], v[56:59]\n"
"ds_read_b128 v[120:123], v2 offset:64512\n"
"v_mfma_f32_16x16x32_bf16 v[60:63], v[144:147], a[236:239], v[60:63]\n"
"ds_read_b128 v[124:127], v3 offset:0\n"
"ds_read_b128 v[140:143], v3 offset:11264\n"
"s_waitcnt lgkmcnt(7)\n"
"v_mfma_f32_16x16x32_bf16 v[64:67], v[148:151], a[232:235], v[64:67]\n"
"ds_read_b128 v[128:131], v3 offset:1024\n"
"v_mfma_f32_16x16x32_bf16 v[68:71], v[148:151], a[236:239], v[68:71]\n"
"ds_read_b128 v[144:147], v3 offset:5120\n"
"s_waitcnt lgkmcnt(8)\n"
"v_mfma_f32_16x16x32_bf16 v[72:75], v[152:155], a[232:235], v[72:75]\n"
"v_mfma_f32_16x16x32_bf16 v[76:79], v[152:155], a[236:239], v[76:79]\n"
"ds_read_b128 v[148:151], v3 offset:6144\n"
"s_waitcnt lgkmcnt(8)\n"
"v_mfma_f32_16x16x32_bf16 v[80:83], v[156:159], a[232:235], v[80:83]\n"
"v_mfma_f32_16x16x32_bf16 v[84:87], v[156:159], a[236:239], v[84:87]\n"
"ds_read_b128 v[152:155], v3 offset:7168\n"
"ds_read_b128 v[156:159], v3 offset:8192\n"
"s_waitcnt vmcnt(5) lgkmcnt(9)\n"
"PAD6_%=:\n"
"s_cbranch_execz PADRET2_%=\n"
"v_mfma_f32_16x16x32_bf16 v[104:107], v[112:115], a[240:243], v[104:107]\n"
"s_waitcnt vmcnt(4)\n"
"v_mfma_f32_16x16x32_bf16 v[108:111], v[112:115], a[244:247], v[108:111]\n"
"ds_read_b128 v[132:135], v1 offset:4096\n"
"s_waitcnt vmcnt(3) lgkmcnt(6)\n"
"v_mfma_f32_16x16x32_bf16 v[104:107], v[140:143], a[248:251], v[104:107]\n"
"ds_read_b128 v[136:139], v1 offset:5120\n"
"s_waitcnt vmcnt(2)\n"
"v_mfma_f32_16x16x32_bf16 v[108:111], v[140:143], a[252:255], v[108:111]\n"
"v_mfma_f32_16x16x32_bf16 v[56:59], v[116:119], a[240:243], v[56:59]\n"
"v_mfma_f32_16x16x32_bf16 v[60:63], v[116:119], a[244:247], v[60:63]\n"
"s_nop 2\n"
"v_fmac_f32 v104, v192, v16\n"
"v_fmac_f32 v105, v193, v16\n"
"v_fmac_f32 v106, v194, v16\n"
"v_fmac_f32 v107, v195, v16\n"
"s_waitcnt lgkmcnt(5)\n"
"v_mfma_f32_16x16x32_bf16 v[56:59], v[144:147], a[248:251], v[56:59]\n"
"v_fmac_f32 v108, v192, v17\n"
"v_fmac_f32 v109, v193, v17\n"
"v_fmac_f32 v110, v194, v17\n"
"v_fmac_f32 v111, v195, v17\n"
"v_mfma_f32_16x16x32_bf16 v[60:63], v[144:147], a[252:255], v[60:63]\n"
"v_cvt_pk_bf16_f32 v104, v104, v105\n"
"v_cvt_pk_bf16_f32 v105, v106, v107\n"
"v_cvt_pk_bf16_f32 v108, v108, v109\n"
"v_cvt_pk_bf16_f32 v109, v110, v111\n"
"v_mfma_f32_16x16x32_bf16 v[64:67], v[120:123], a[240:243], v[64:67]\n"
"v_pk_max_i16 v104, v104, 0\n"
"v_pk_max_i16 v105, v105, 0\n"
"v_mfma_f32_16x16x32_bf16 v[68:71], v[120:123], a[244:247], v[68:71]\n"
"v_pk_max_i16 v108, v108, 0\n"
"v_pk_max_i16 v109, v109, 0\n"
"buffer_store_dwordx2 v[104:105], v23, s[40:43], s94 offen sc1\n"
"buffer_store_dwordx2 v[108:109], v23, s[40:43], s95 offen sc1\n"
"s_waitcnt lgkmcnt(4)\n"
"v_mfma_f32_16x16x32_bf16 v[64:67], v[148:151], a[248:251], v[64:67]\n"
"v_fmac_f32 v56, v168, v16\n"
"v_fmac_f32 v57, v169, v16\n"
"v_fmac_f32 v58, v170, v16\n"
"v_fmac_f32 v59, v171, v16\n"
"v_mfma_f32_16x16x32_bf16 v[68:71], v[148:151], a[252:255], v[68:71]\n"
"v_fmac_f32 v60, v168, v17\n"
"v_fmac_f32 v61, v169, v17\n"
"v_fmac_f32 v62, v170, v17\n"
"v_fmac_f32 v63, v171, v17\n"
"v_mfma_f32_16x16x32_bf16 v[72:75], v[124:127], a[240:243], v[72:75]\n"
"v_cvt_pk_bf16_f32 v56, v56, v57\n"
"v_cvt_pk_bf16_f32 v57, v58, v59\n"
"v_mfma_f32_16x16x32_bf16 v[76:79], v[124:127], a[244:247], v[76:79]\n"
"v_cvt_pk_bf16_f32 v60, v60, v61\n"
"v_cvt_pk_bf16_f32 v61, v62, v63\n"
"v_pk_max_i16 v56, v56, 0\n"
"v_pk_max_i16 v57, v57, 0\n"
"s_waitcnt lgkmcnt(3)\n"
"v_mfma_f32_16x16x32_bf16 v[72:75], v[152:155], a[248:251], v[72:75]\n"
"v_pk_max_i16 v60, v60, 0\n"
"v_pk_max_i16 v61, v61, 0\n"
"v_fmac_f32 v64, v172, v16\n"
"v_fmac_f32 v65, v173, v16\n"
"v_mfma_f32_16x16x32_bf16 v[76:79], v[152:155], a[252:255], v[76:79]\n"
"v_fmac_f32 v66, v174, v16\n"
"v_fmac_f32 v67, v175, v16\n"
"v_fmac_f32 v68, v172, v17\n"
"v_fmac_f32 v69, v173, v17\n"
"v_mfma_f32_16x16x32_bf16 v[80:83], v[128:131], a[240:243], v[80:83]\n"
"v_fmac_f32 v70, v174, v17\n"
"v_fmac_f32 v71, v175, v17\n"
"v_mfma_f32_16x16x32_bf16 v[84:87], v[128:131], a[244:247], v[84:87]\n"
"v_cvt_pk_bf16_f32 v58, v64, v65\n"
"v_cvt_pk_bf16_f32 v59, v66, v67\n"
"v_cvt_pk_bf16_f32 v62, v68, v69\n"
"v_cvt_pk_bf16_f32 v63, v70, v71\n"
"s_waitcnt lgkmcnt(2)\n"
"v_mfma_f32_16x16x32_bf16 v[80:83], v[156:159], a[248:251], v[80:83]\n"
"v_pk_max_i16 v58, v58, 0\n"
"v_pk_max_i16 v59, v59, 0\n"
"v_pk_max_i16 v62, v62, 0\n"
"v_pk_max_i16 v63, v63, 0\n"
"v_mfma_f32_16x16x32_bf16 v[84:87], v[156:159], a[252:255], v[84:87]\n"
"buffer_store_dwordx4 v[56:59], v1, s[40:43], s92 offen offset:0 sc1\n"
"buffer_store_dwordx4 v[60:63], v1, s[40:43], s93 offen offset:0 sc1\n"
"v_fmac_f32 v72, v176, v16\n"
"v_fmac_f32 v73, v177, v16\n"
"s_waitcnt lgkmcnt(1)\n"
"v_mfma_f32_16x16x32_bf16 v[88:91], v[132:135], a[96:99], 0\n"
"ds_read_b128 v[160:163], v1 offset:11264\n"
"v_mfma_f32_16x16x32_bf16 v[92:95], v[132:135], a[100:103], 0\n"
"ds_read_b128 v[164:167], v1 offset:12288\n"
"v_fmac_f32 v74, v178, v16\n"
"v_fmac_f32 v75, v179, v16\n"
"s_waitcnt lgkmcnt(2)\n"
"v_mfma_f32_16x16x32_bf16 v[96:99], v[136:139], a[96:99], 0\n"
"v_fmac_f32 v76, v176, v17\n"
"v_fmac_f32 v77, v177, v17\n"
"v_fmac_f32 v78, v178, v17\n"
"v_mfma_f32_16x16x32_bf16 v[100:103], v[136:139], a[100:103], 0\n"
"v_fmac_f32 v79, v179, v17\n"
"v_cvt_pk_bf16_f32 v72, v72, v73\n"
"v_cvt_pk_bf16_f32 v73, v74, v75\n"
"s_waitcnt lgkmcnt(1)\n"
"v_mfma_f32_16x16x32_bf16 v[88:91], v[160:163], a[104:107], v[88:91]\n"
"ds_read_b128 v[132:135], v1 offset:18432\n"
"v_cvt_pk_bf16_f32 v76, v76, v77\n"
"v_cvt_pk_bf16_f32 v77, v78, v79\n"
"v_mfma_f32_16x16x32_bf16 v[92:95], v[160:163], a[108:111], v[92:95]\n"
"ds_read_b128 v[136:139], v1 offset:19456\n"
"v_pk_max_i16 v72, v72, 0\n"
"v_pk_max_i16 v73, v73, 0\n"
"s_waitcnt lgkmcnt(2)\n"
"v_mfma_f32_16x16x32_bf16 v[96:99], v[164:167], a[104:107], v[96:99]\n"
"v_pk_max_i16 v76, v76, 0\n"
"v_pk_max_i16 v77, v77, 0\n"
"v_fmac_f32 v80, v180, v16\n"
"v_mfma_f32_16x16x32_bf16 v[100:103], v[164:167], a[108:111], v[100:103]\n"
"v_fmac_f32 v81, v181, v16\n"
"v_fmac_f32 v82, v182, v16\n"
"v_fmac_f32 v83, v183, v16\n"
"s_waitcnt lgkmcnt(1)\n"
"v_mfma_f32_16x16x32_bf16 v[88:91], v[132:135], a[112:115], v[88:91]\n"
"ds_read_b128 v[160:163], v1 offset:25600\n"
"v_fmac_f32 v84, v180, v17\n"
"v_fmac_f32 v85, v181, v17\n"
"v_mfma_f32_16x16x32_bf16 v[92:95], v[132:135], a[116:119], v[92:95]\n"
"ds_read_b128 v[164:167], v1 offset:26624\n"
"v_fmac_f32 v86, v182, v17\n"
"v_fmac_f32 v87, v183, v17\n"
"s_waitcnt lgkmcnt(2)\n"
"v_mfma_f32_16x16x32_bf16 v[96:99], v[136:139], a[112:115], v[96:99]\n"
"v_cvt_pk_bf16_f32 v74, v80, v81\n"
"v_cvt_pk_bf16_f32 v75, v82, v83\n"
"v_cvt_pk_bf16_f32 v78, v84, v85\n"
"v_mfma_f32_16x16x32_bf16 v[100:103], v[136:139], a[116:119], v[100:103]\n"
"v_cvt_pk_bf16_f32 v79, v86, v87\n"
"v_pk_max_i16 v74, v74, 0\n"
"v_pk_max_i16 v75, v75, 0\n"
"s_waitcnt lgkmcnt(1)\n"
"v_mfma_f32_16x16x32_bf16 v[88:91], v[160:163], a[120:123], v[88:91]\n"
"ds_read_b128 v[132:135], v1 offset:32768\n"
"v_pk_max_i16 v78, v78, 0\n"
"v_pk_max_i16 v79, v79, 0\n"
"v_mfma_f32_16x16x32_bf16 v[92:95], v[160:163], a[124:127], v[92:95]\n"
"ds_read_b128 v[136:139], v1 offset:33792\n"
"buffer_store_dwordx4 v[72:75], v1, s[40:43], s92 offen offset:1024 sc1\n"
"buffer_store_dwordx4 v[76:79], v1, s[40:43], s93 offen offset:1024 sc1\n"
"s_waitcnt lgkmcnt(2)\n"
"v_mfma_f32_16x16x32_bf16 v[96:99], v[164:167], a[120:123], v[96:99]\n"
"s_add_u32 s46, s47, 65536\n"
"buffer_load_dwordx4 a[96:99], v1, s[40:43], s46 offen offset:0 sc1\n"
"v_mfma_f32_16x16x32_bf16 v[100:103], v[164:167], a[124:127], v[100:103]\n"
"s_waitcnt lgkmcnt(1)\n"
"v_mfma_f32_16x16x32_bf16 v[88:91], v[132:135], a[128:131], v[88:91]\n"
"ds_read_b128 v[160:163], v1 offset:39936\n"
"v_mfma_f32_16x16x32_bf16 v[92:95], v[132:135], a[132:135], v[92:95]\n"
"ds_read_b128 v[164:167], v1 offset:40960\n"
"s_waitcnt lgkmcnt(2)\n"
"v_mfma_f32_16x16x32_bf16 v[96:99], v[136:139], a[128:131], v[96:99]\n"
"s_add_u32 s46, s21, 49152\n"
"buffer_load_dwordx4 a[100:103], v20, s[40:43], s46 offen offset:2048 sc1\n"
"v_mfma_f32_16x16x32_bf16 v[100:103], v[136:139], a[132:135], v[100:103]\n"
"s_waitcnt lgkmcnt(1)\n"
"v_mfma_f32_16x16x32_bf16 v[88:91], v[160:163], a[136:139], v[88:91]\n"
"ds_read_b128 v[132:135], v1 offset:47104\n"
"v_mfma_f32_16x16x32_bf16 v[92:95], v[160:163], a[140:143], v[92:95]\n"
"ds_read_b128 v[136:139], v1 offset:48128\n"
"s_waitcnt lgkmcnt(2)\n"
"v_mfma_f32_16x16x32_bf16 v[96:99], v[164:167], a[136:139], v[96:99]\n"
"s_add_u32 s46, s21, 49152\n"
"buffer_load_dwordx4 a[104:107], v20, s[40:43], s46 offen offset:3072 sc1\n"
"v_mfma_f32_16x16x32_bf16 v[100:103], v[164:167], a[140:143], v[100:103]\n"
"s_waitcnt lgkmcnt(1)\n"
"v_mfma_f32_16x16x32_bf16 v[88:91], v[132:135], a[144:147], v[88:91]\n"
"ds_read_b128 v[160:163], v1 offset:54272\n"
"v_mfma_f32_16x16x32_bf16 v[92:95], v[132:135], a[148:151], v[92:95]\n"
"ds_read_b128 v[164:167], v1 offset:55296\n"
"s_waitcnt lgkmcnt(2)\n"
"v_mfma_f32_16x16x32_bf16 v[96:99], v[136:139], a[144:147], v[96:99]\n"
"s_add_u32 s46, s21, 53248\n"
"buffer_load_dwordx4 a[108:111], v20, s[40:43], s46 offen offset:0 sc1\n"
"v_mfma_f32_16x16x32_bf16 v[100:103], v[136:139], a[148:151], v[100:103]\n"
"s_waitcnt lgkmcnt(1)\n"
"v_mfma_f32_16x16x32_bf16 v[88:91], v[160:163], a[152:155], v[88:91]\n"
"ds_read_b128 v[132:135], v1 offset:61440\n"
"v_mfma_f32_16x16x32_bf16 v[92:95], v[160:163], a[156:159], v[92:95]\n"
"ds_read_b128 v[136:139], v1 offset:62464\n"
"s_waitcnt lgkmcnt(2)\n"
"v_mfma_f32_16x16x32_bf16 v[96:99], v[164:167], a[152:155], v[96:99]\n"
"s_add_u32 s46, s21, 53248\n"
"buffer_load_dwordx4 a[112:115], v20, s[40:43], s46 offen offset:1024 sc1\n"
"v_mfma_f32_16x16x32_bf16 v[100:103], v[164:167], a[156:159], v[100:103]\n"
"s_waitcnt lgkmcnt(1)\n"
"v_mfma_f32_16x16x32_bf16 v[88:91], v[132:135], a[160:163], v[88:91]\n"
"ds_read_b128 v[160:163], v2 offset:3072\n"
"v_mfma_f32_16x16x32_bf16 v[92:95], v[132:135], a[164:167], v[92:95]\n"
"ds_read_b128 v[164:167], v2 offset:4096\n"
"s_waitcnt lgkmcnt(2)\n"
"v_mfma_f32_16x16x32_bf16 v[96:99], v[136:139], a[160:163], v[96:99]\n"
"s_add_u32 s46, s21, 53248\n"
"buffer_load_dwordx4 a[116:119], v20, s[40:43], s46 offen offset:2048 sc1\n"
"v_mfma_f32_16x16x32_bf16 v[100:103], v[136:139], a[164:167], v[100:103]\n"
"s_waitcnt lgkmcnt(1)\n"
"v_mfma_f32_16x16x32_bf16 v[88:91], v[160:163], a[168:171], v[88:91]\n"
"ds_read_b128 v[132:135], v2 offset:10240\n"
"v_mfma_f32_16x16x32_bf16 v[92:95], v[160:163], a[172:175], v[92:95]\n"
"ds_read_b128 v[136:139], v2 offset:11264\n"
"s_waitcnt lgkmcnt(2)\n"
"v_mfma_f32_16x16x32_bf16 v[96:99], v[164:167], a[168:171], v[96:99]\n"
"s_add_u32 s46, s21, 12288\n"
"buffer_load_dwordx4 a[120:123], v1, s[40:43], s46 offen offset:3072 sc1\n"
"v_mfma_f32_16x16x32_bf16 v[100:103], v[164:167], a[172:175], v[100:103]\n"
"s_waitcnt lgkmcnt(1)\n"
"v_mfma_f32_16x16x32_bf16 v[88:91], v[132:135], a[176:179], v[88:91]\n"
"ds_read_b128 v[160:163], v2 offset:17408\n"
"v_mfma_f32_16x16x32_bf16 v[92:95], v[132:135], a[180:183], v[92:95]\n"
"ds_read_b128 v[164:167], v2 offset:18432\n"
"s_waitcnt lgkmcnt(2)\n"
"v_mfma_f32_16x16x32_bf16 v[96:99], v[136:139], a[176:179], v[96:99]\n"
"s_add_u32 s46, s21, 16384\n"
"buffer_load_dwordx4 a[124:127], v1, s[40:43], s46 offen offset:0 sc1\n"
"v_mfma_f32_16x16x32_bf16 v[100:103], v[136:139], a[180:183], v[100:103]\n"
"s_waitcnt lgkmcnt(1)\n"
"v_mfma_f32_16x16x32_bf16 v[88:91], v[160:163], a[184:187], v[88:91]\n"
"ds_read_b128 v[132:135], v2 offset:24576\n"
"v_mfma_f32_16x16x32_bf16 v[92:95], v[160:163], a[188:191], v[92:95]\n"
"ds_read_b128 v[136:139], v2 offset:25600\n"
"s_waitcnt lgkmcnt(2)\n"
"v_mfma_f32_16x16x32_bf16 v[96:99], v[164:167], a[184:187], v[96:99]\n"
"s_add_u32 s46, s21, 16384\n"
"buffer_load_dwordx4 a[128:131], v1, s[40:43], s46 offen offset:1024 sc1\n"
"v_mfma_f32_16x16x32_bf16 v[100:103], v[164:167], a[188:191], v[100:103]\n"
"s_waitcnt lgkmcnt(1)\n"
"v_mfma_f32_16x16x32_bf16 v[88:91], v[132:135], a[192:195], v[88:91]\n"
"ds_read_b128 v[160:163], v2 offset:31744\n"
"v_mfma_f32_16x16x32_bf16 v[92:95], v[132:135], a[196:199], v[92:95]\n"
"ds_read_b128 v[164:167], v2 offset:32768\n"
"s_waitcnt lgkmcnt(2)\n"
"v_mfma_f32_16x16x32_bf16 v[96:99], v[136:139], a[192:195], v[96:99]\n"
"s_add_u32 s46, s21, 16384\n"
"buffer_load_dwordx4 a[132:135], v1, s[40:43], s46 offen offset:2048 sc1\n"
"v_mfma_f32_16x16x32_bf16 v[100:103], v[136:139], a[196:199], v[100:103]\n"
"s_waitcnt lgkmcnt(1)\n"
"v_mfma_f32_16x16x32_bf16 v[88:91], v[160:163], a[200:203], v[88:91]\n"
"ds_read_b128 v[132:135], v2 offset:38912\n"
"v_mfma_f32_16x16x32_bf16 v[92:95], v[160:163], a[204:207], v[92:95]\n"
"ds_read_b128 v[136:139], v2 offset:39936\n"
"s_waitcnt lgkmcnt(2)\n"
"v_mfma_f32_16x16x32_bf16 v[96:99], v[164:167], a[200:203], v[96:99]\n"
"s_add_u32 s46, s21, 16384\n"
"buffer_load_dwordx4 a[136:139], v1, s[40:43], s46 offen offset:3072 sc1\n"
"v_mfma_f32_16x16x32_bf16 v[100:103], v[164:167], a[204:207], v[100:103]\n"
"s_waitcnt lgkmcnt(1)\n"
"v_mfma_f32_16x16x32_bf16 v[88:91], v[132:135], a[208:211], v[88:91]\n"
"ds_read_b128 v[160:163], v2 offset:46080\n"
"v_mfma_f32_16x16x32_bf16 v[92:95], v[132:135], a[212:215], v[92:95]\n"
"ds_read_b128 v[164:167], v2 offset:47104\n"
"s_waitcnt lgkmcnt(2)\n"
"v_mfma_f32_16x16x32_bf16 v[96:99], v[136:139], a[208:211], v[96:99]\n"
"s_add_u32 s46, s21, 8192\n"
"buffer_load_dwordx4 a[140:143], v1, s[40:43], s46 offen offset:2048 sc1\n"
"v_mfma_f32_16x16x32_bf16 v[100:103], v[136:139], a[212:215], v[100:103]\n"
"s_waitcnt lgkmcnt(1)\n"
"v_mfma_f32_16x16x32_bf16 v[88:91], v[160:163], a[216:219], v[88:91]\n"
"ds_read_b128 v[132:135], v2 offset:53248\n"
"v_mfma_f32_16x16x32_bf16 v[92:95], v[160:163], a[220:223], v[92:95]\n"
"ds_read_b128 v[136:139], v2 offset:54272\n"
"s_waitcnt lgkmcnt(2)\n"
"v_mfma_f32_16x16x32_bf16 v[96:99], v[164:167], a[216:219], v[96:99]\n"
"s_add_u32 s46, s21, 8192\n"
"buffer_load_dwordx4 a[144:147], v1, s[40:43], s46 offen offset:3072 sc1\n"
"v_mfma_f32_16x16x32_bf16 v[100:103], v[164:167], a[220:223], v[100:103]\n"
"s_waitcnt vmcnt(13)\n"
"s_mov_b64 exec, 1\n"
"buffer_store_dwordx2 v[4:5], off, s[40:43], s71 sc1\n"
"s_mov_b64 exec, -1\n"
"s_add_u32 s71, s71, 32768\n"
"s_waitcnt lgkmcnt(1)\n"
"v_mfma_f32_16x16x32_bf16 v[88:91], v[132:135], a[224:227], v[88:91]\n"
"ds_read_b128 v[160:163], v2 offset:60416\n"
"v_mfma_f32_16x16x32_bf16 v[92:95], v[132:135], a[228:231], v[92:95]\n"
"ds_read_b128 v[164:167], v2 offset:61440\n"
"s_waitcnt lgkmcnt(2)\n"
"v_mfma_f32_16x16x32_bf16 v[96:99], v[136:139], a[224:227], v[96:99]\n"
"s_add_u32 s46, s21, 12288\n"
"buffer_load_dwordx4 a[148:151], v1, s[40:43], s46 offen offset:0 sc1\n"
"v_mfma_f32_16x16x32_bf16 v[100:103], v[136:139], a[228:231], v[100:103]\n"
"s_waitcnt lgkmcnt(1)\n"
"v_mfma_f32_16x16x32_bf16 v[88:91], v[160:163], a[232:235], v[88:91]\n"
"ds_read_b128 v[132:135], v3 offset:2048\n"
"v_mfma_f32_16x16x32_bf16 v[92:95], v[160:163], a[236:239], v[92:95]\n"
"ds_read_b128 v[136:139], v3 offset:3072\n"
"s_waitcnt lgkmcnt(2)\n"
"v_mfma_f32_16x16x32_bf16 v[96:99], v[164:167], a[232:235], v[96:99]\n"
"v_mfma_f32_16x16x32_bf16 v[100:103], v[164:167], a[236:239], v[100:103]\n"
"ds_read_b128 v[160:163], v3 offset:9216\n"
"ds_read_b128 v[164:167], v3 offset:10240\n"
"s_waitcnt lgkmcnt(3)\n"
"v_mfma_f32_16x16x32_bf16 v[88:91], v[132:135], a[240:243], v[88:91]\n"
"v_mfma_f32_16x16x32_bf16 v[92:95], v[132:135], a[244:247], v[92:95]\n"
"s_waitcnt lgkmcnt(1)\n"
"v_mfma_f32_16x16x32_bf16 v[88:91], v[160:163], a[248:251], v[88:91]\n"
"v_mfma_f32_16x16x32_bf16 v[92:95], v[160:163], a[252:255], v[92:95]\n"
"v_mfma_f32_16x16x32_bf16 v[96:99], v[136:139], a[240:243], v[96:99]\n"
"v_mfma_f32_16x16x32_bf16 v[100:103], v[136:139], a[244:247], v[100:103]\n"
"s_nop 4\n"
"v_fmac_f32 v88, v184, v16\n"
"v_fmac_f32 v89, v185, v16\n"
"v_fmac_f32 v90, v186, v16\n"
"v_fmac_f32 v91, v187, v16\n"
"s_waitcnt lgkmcnt(0)\n"
"v_mfma_f32_16x16x32_bf16 v[96:99], v[164:167], a[248:251], v[96:99]\n"
"v_fmac_f32 v92, v184, v17\n"
"v_fmac_f32 v93, v185, v17\n"
"v_fmac_f32 v94, v186, v17\n"
"v_fmac_f32 v95, v187, v17\n"
"v_mfma_f32_16x16x32_bf16 v[100:103], v[164:167], a[252:255], v[100:103]\n"
"v_cvt_pk_bf16_f32 v88, v88, v89\n"
"v_cvt_pk_bf16_f32 v89, v90, v91\n"
"v_cvt_pk_bf16_f32 v92, v92, v93\n"
"v_cvt_pk_bf16_f32 v93, v94, v95\n"
"v_pk_max_i16 v88, v88, 0\n"
"v_pk_max_i16 v89, v89, 0\n"
"v_pk_max_i16 v92, v92, 0\n"
"v_pk_max_i16 v93, v93, 0\n"
"v_fmac_f32 v96, v188, v16\n"
"v_fmac_f32 v97, v189, v16\n"
"v_fmac_f32 v98, v190, v16\n"
"v_fmac_f32 v99, v191, v16\n"
"v_fmac_f32 v100, v188, v17\n"
"v_fmac_f32 v101, v189, v17\n"
"v_fmac_f32 v102, v190, v17\n"
"v_fmac_f32 v103, v191, v17\n"
"v_cvt_pk_bf16_f32 v90, v96, v97\n"
"v_cvt_pk_bf16_f32 v91, v98, v99\n"
"v_cvt_pk_bf16_f32 v94, v100, v101\n"
"v_cvt_pk_bf16_f32 v95, v102, v103\n"
"v_pk_max_i16 v90, v90, 0\n"
"v_pk_max_i16 v91, v91, 0\n"
"v_pk_max_i16 v94, v94, 0\n"
"v_pk_max_i16 v95, v95, 0\n"
"buffer_store_dwordx4 v[88:91], v1, s[40:43], s92 offen offset:2048 sc1\n"
"buffer_store_dwordx4 v[92:95], v1, s[40:43], s93 offen offset:2048 sc1\n"
"s_add_u32 s46, s21, 12288\n"
"buffer_load_dwordx4 a[152:155], v1, s[40:43], s46 offen offset:1024 sc1\n"
"s_nop 13\n"
"s_add_u32 s46, s21, 12288\n"
"buffer_load_dwordx4 a[156:159], v1, s[40:43], s46 offen offset:2048 sc1\n"
"s_nop 13\n"
"s_add_u32 s46, s21, 28672\n"
"buffer_load_dwordx4 a[160:163], v1, s[40:43], s46 offen offset:2048 sc1\n"
"s_nop 13\n"
"s_add_u32 s46, s21, 28672\n"
"buffer_load_dwordx4 a[164:167], v1, s[40:43], s46 offen offset:3072 sc1\n"
"s_nop 13\n"
"s_add_u32 s46, s21, 32768\n"
"buffer_load_dwordx4 a[168:171], v1, s[40:43], s46 offen offset:0 sc1\n"
"s_nop 13\n"
"s_add_u32 s46, s21, 32768\n"
"buffer_load_dwordx4 a[172:175], v1, s[40:43], s46 offen offset:1024 sc1\n"
"s_nop 13\n"
"s_add_u32 s46, s21, 32768\n"
"buffer_load_dwordx4 a[176:179], v1, s[40:43], s46 offen offset:2048 sc1\n"
"s_nop 13\n"
"s_add_u32 s46, s21, 45056\n"
"buffer_load_dwordx4 a[180:183], v1, s[40:43], s46 offen offset:1024 sc1\n"
"s_nop 13\n"
"s_add_u32 s46, s21, 45056\n"
"buffer_load_dwordx4 a[184:187], v1, s[40:43], s46 offen offset:2048 sc1\n"
"s_nop 13\n"
"s_add_u32 s46, s21, 45056\n"
"buffer_load_dwordx4 a[188:191], v1, s[40:43], s46 offen offset:3072 sc1\n"
"s_nop 13\n"
"s_add_u32 s46, s21, 49152\n"
"buffer_load_dwordx4 a[192:195], v1, s[40:43], s46 offen offset:0 sc1\n"
"s_nop 13\n"
"s_mov_b64 exec, 0xff\n"
"buffer_load_dwordx2 v[44:45], v22, s[40:43], s77 offen sc1\n"
"s_mov_b64 exec, -1\n"
"s_add_u32 s46, s21, 49152\n"
"buffer_load_dwordx4 a[196:199], v1, s[40:43], s46 offen offset:1024 sc1\n"
"s_nop 13\n"
"s_add_u32 s46, s47, 65536\n"
"buffer_load_dwordx4 a[200:203], v1, s[40:43], s46 offen offset:1024 sc1\n"
"s_nop 13\n"
"s_add_u32 s46, s47, 65536\n"
"buffer_load_dwordx4 a[204:207], v1, s[40:43], s46 offen offset:2048 sc1\n"
"s_nop 13\n"
"s_add_u32 s46, s47, 65536\n"
"buffer_load_dwordx4 a[208:211], v1, s[40:43], s46 offen offset:3072 sc1\n"
"s_nop 13\n"
"s_add_u32 s46, s47, 69632\n"
"buffer_load_dwordx4 a[212:215], v1, s[40:43], s46 offen offset:0 sc1\n"
"s_nop 13\n"
"s_add_u32 s46, s47, 69632\n"
"buffer_load_dwordx4 a[216:219], v1, s[40:43], s46 offen offset:1024 sc1\n"
"s_barrier\n"
"s_mov_b64 exec, 0xff\n"
"s_mov_b32 s67, 262144\n"
"s_waitcnt vmcnt(6)\n"
"v_cmp_ne_u64 vcc, s[56:57], v[44:45]\n"
"s_nop 3\n"
"s_cbranch_vccz PD27_%=\n"
"PL26_%=:\n"
"s_sleep 1\n"
"buffer_load_dwordx2 v[44:45], v22, s[40:43], s77 offen sc1\n"
"s_waitcnt vmcnt(0)\n"
"v_cmp_ne_u64 vcc, s[56:57], v[44:45]\n"
"s_nop 3\n"
"s_cbranch_vccz PD27_%=\n"
"s_sub_u32 s67, s67, 1\n"
"s_cbranch_scc0 PL26_%=\n"
"PD27_%=:\n"
"s_mov_b64 exec, -1\n"
"s_add_u32 s77, s77, 32768\n"
"buffer_load_dwordx4 v[136:139], v21, s[40:43], s68 offen offset:0 sc1\n"
"buffer_load_dwordx4 v[140:143], v21, s[40:43], s69 offen offset:0 sc1\n"
"s_mov_b64 exec, 1\n"
"buffer_store_dwordx2 v[4:5], off, s[40:43], s71 sc1\n"
"s_mov_b64 exec, -1\n"
"buffer_load_dwordx4 v[144:147], v21, s[40:43], s72 offen offset:0 sc1\n"
"s_nop 7\n"
"buffer_load_dwordx4 v[148:151], v21, s[40:43], s73 offen offset:0 sc1\n"
"s_nop 7\n"
"buffer_load_dwordx4 v[152:155], v21, s[40:43], s68 offen offset:1024 sc1\n"
"s_nop 7\n"
"buffer_load_dwordx4 v[156:159], v21, s[40:43], s69 offen offset:1024 sc1\n"
"s_nop 7\n"
"buffer_load_dwordx4 v[160:163], v21, s[40:43], s72 offen offset:1024 sc1\n"
"s_nop 7\n"
"buffer_load_dwordx4 v[164:167], v21, s[40:43], s73 offen offset:1024 sc1\n"
"s_nop 7\n"
"s_add_u32 s68, s68, 4096\n"
"buffer_load_dwordx4 v[168:171], v21, s[40:43], s68 offen offset:0 sc1\n"
"s_nop 7\n"
"s_add_u32 s69, s69, 4096\n"
"buffer_load_dwordx4 v[172:175], v21, s[40:43], s69 offen offset:0 sc1\n"
"s_nop 7\n"
"s_add_u32 s72, s72, 4096\n"
"buffer_load_dwordx4 v[176:179], v21, s[40:43], s72 offen offset:0 sc1\n"
"s_nop 7\n"
"s_add_u32 s73, s73, 4096\n"
"buffer_load_dwordx4 v[180:183], v21, s[40:43], s73 offen offset:0 sc1\n"
"s_waitcnt vmcnt(12)\n"
"v_mfma_f32_16x16x32_bf16 v[56:59], v[196:199], v[136:139], 0\n"
"v_mfma_f32_16x16x32_bf16 v[60:63], v[200:203], v[136:139], 0\n"
"buffer_load_dwordx4 v[184:187], v21, s[40:43], s68 offen offset:1024 sc1\n"
"v_mfma_f32_16x16x32_bf16 v[64:67], v[204:207], v[136:139], 0\n"
"v_mfma_f32_16x16x32_bf16 v[68:71], v[208:211], v[136:139], 0\n"
"buffer_load_dwordx4 v[188:191], v21, s[40:43], s69 offen offset:1024 sc1\n"
"v_mfma_f32_16x16x32_bf16 v[72:75], v[212:215], v[136:139], 0\n"
"s_waitcnt vmcnt(13)\n"
"v_mfma_f32_16x16x32_bf16 v[76:79], v[196:199], v[140:143], 0\n"
"v_mfma_f32_16x16x32_bf16 v[80:83], v[200:203], v[140:143], 0\n"
"buffer_load_dwordx4 v[192:195], v21, s[40:43], s72 offen offset:1024 sc1\n"
"v_mfma_f32_16x16x32_bf16 v[84:87], v[204:207], v[140:143], 0\n"
"v_mfma_f32_16x16x32_bf16 v[88:91], v[208:211], v[140:143], 0\n"
"buffer_load_dwordx4 v[136:139], v21, s[40:43], s73 offen offset:1024 sc1\n"
"v_mfma_f32_16x16x32_bf16 v[92:95], v[212:215], v[140:143], 0\n"
"s_waitcnt vmcnt(13)\n"
"v_mfma_f32_16x16x32_bf16 v[96:99], v[196:199], v[144:147], 0\n"
"v_mfma_f32_16x16x32_bf16 v[100:103], v[200:203], v[144:147], 0\n"
"buffer_load_dwordx4 v[140:143], v21, s[40:43], s68 offen offset:3072 sc1\n"
"v_mfma_f32_16x16x32_bf16 v[104:107], v[204:207], v[144:147], 0\n"
"v_mfma_f32_16x16x32_bf16 v[108:111], v[208:211], v[144:147], 0\n"
"v_mfma_f32_16x16x32_bf16 v[112:115], v[212:215], v[144:147], 0\n"
"s_waitcnt vmcnt(13)\n"
"v_mfma_f32_16x16x32_bf16 v[116:119], v[196:199], v[148:151], 0\n"
"v_mfma_f32_16x16x32_bf16 v[120:123], v[200:203], v[148:151], 0\n"
"buffer_load_dwordx4 v[144:147], v21, s[40:43], s69 offen offset:3072 sc1\n"
"v_mfma_f32_16x16x32_bf16 v[124:127], v[204:207], v[148:151], 0\n"
"v_mfma_f32_16x16x32_bf16 v[128:131], v[208:211], v[148:151], 0\n"
"v_mfma_f32_16x16x32_bf16 v[132:135], v[212:215], v[148:151], 0\n"
"s_waitcnt vmcnt(13)\n"
"v_mfma_f32_16x16x32_bf16 v[56:59], v[216:219], v[152:155], v[56:59]\n"
"v_mfma_f32_16x16x32_bf16 v[60:63], v[220:223], v[152:155], v[60:63]\n"
"buffer_load_dwordx4 v[196:199], v21, s[40:43], s72 offen offset:3072 sc1\n"
"v_mfma_f32_16x16x32_bf16 v[64:67], v[224:227], v[152:155], v[64:67]\n"
"v_mfma_f32_16x16x32_bf16 v[68:71], v[228:231], v[152:155], v[68:71]\n"
"v_mfma_f32_16x16x32_bf16 v[72:75], v[232:235], v[152:155], v[72:75]\n"
"s_waitcnt vmcnt(13)\n"
"v_mfma_f32_16x16x32_bf16 v[76:79], v[216:219], v[156:159], v[76:79]\n"
"v_mfma_f32_16x16x32_bf16 v[80:83], v[220:223], v[156:159], v[80:83]\n"
"s_add_u32 s68, s68, 4096\n"
"buffer_load_dwordx4 v[200:203], v21, s[40:43], s68 offen offset:0 sc1\n"
"v_mfma_f32_16x16x32_bf16 v[84:87], v[224:227], v[156:159], v[84:87]\n"
"v_mfma_f32_16x16x32_bf16 v[88:91], v[228:231], v[156:159], v[88:91]\n"
"v_mfma_f32_16x16x32_bf16 v[92:95], v[232:235], v[156:159], v[92:95]\n"
"s_waitcnt vmcnt(13)\n"
"v_mfma_f32_16x16x32_bf16 v[96:99], v[216:219], v[160:163], v[96:99]\n"
"v_mfma_f32_16x16x32_bf16 v[100:103], v[220:223], v[160:163], v[100:103]\n"
"s_add_u32 s69, s69, 4096\n"
"buffer_load_dwordx4 v[204:207], v21, s[40:43], s69 offen offset:0 sc1\n"
"v_mfma_f32_16x16x32_bf16 v[104:107], v[224:227], v[160:163], v[104:107]\n"
"v_mfma_f32_16x16x32_bf16 v[108:111], v[228:231], v[160:163], v[108:111]\n"
"v_mfma_f32_16x16x32_bf16 v[112:115], v[232:235], v[160:163], v[112:115]\n"
"s_waitcnt vmcnt(13)\n"
"v_mfma_f32_16x16x32_bf16 v[116:119], v[216:219], v[164:167], v[116:119]\n"
"v_mfma_f32_16x16x32_bf16 v[120:123], v[220:223], v[164:167], v[120:123]\n"
"s_add_u32 s72, s72, 4096\n"
"buffer_load_dwordx4 v[208:211], v21, s[40:43], s72 offen offset:0 sc1\n"
"v_mfma_f32_16x16x32_bf16 v[124:127], v[224:227], v[164:167], v[124:127]\n"
"v_mfma_f32_16x16x32_bf16 v[128:131], v[228:231], v[164:167], v[128:131]\n"
"v_mfma_f32_16x16x32_bf16 v[132:135], v[232:235], v[164:167], v[132:135]\n"
"s_mov_b64 exec, 0xff\n"
"buffer_load_dwordx2 v[44:45], v22, s[40:43], s77 offen sc1\n"
"s_mov_b64 exec, -1\n"
"s_waitcnt vmcnt(14)\n"
"v_mfma_f32_16x16x32_bf16 v[56:59], v[236:239], v[168:171], v[56:59]\n"
"v_mfma_f32_16x16x32_bf16 v[60:63], v[240:243], v[168:171], v[60:63]\n"
"buffer_load_dwordx4 v[212:215], v1, s[40:43], s68 offen offset:3072 sc1\n"
"v_mfma_f32_16x16x32_bf16 v[64:67], v[244:247], v[168:171], v[64:67]\n"
"v_mfma_f32_16x16x32_bf16 v[68:71], v[248:251], v[168:171], v[68:71]\n"
"v_mfma_f32_16x16x32_bf16 v[72:75], v[252:255], v[168:171], v[72:75]\n"
"s_waitcnt vmcnt(14)\n"
"v_mfma_f32_16x16x32_bf16 v[76:79], v[236:239], v[172:175], v[76:79]\n"
"v_mfma_f32_16x16x32_bf16 v[80:83], v[240:243], v[172:175], v[80:83]\n"
"buffer_load_dwordx4 v[148:151], v1, s[40:43], s69 offen offset:3072 sc1\n"
"v_mfma_f32_16x16x32_bf16 v[84:87], v[244:247], v[172:175], v[84:87]\n"
"v_mfma_f32_16x16x32_bf16 v[88:91], v[248:251], v[172:175], v[88:91]\n"
"v_mfma_f32_16x16x32_bf16 v[92:95], v[252:255], v[172:175], v[92:95]\n"
"s_waitcnt vmcnt(14)\n"
"v_mfma_f32_16x16x32_bf16 v[96:99], v[236:239], v[176:179], v[96:99]\n"
"v_mfma_f32_16x16x32_bf16 v[100:103], v[240:243], v[176:179], v[100:103]\n"
"buffer_load_dwordx4 v[152:155], v1, s[40:43], s72 offen offset:3072 sc1\n"
"v_mfma_f32_16x16x32_bf16 v[104:107], v[244:247], v[176:179], v[104:107]\n"
"v_mfma_f32_16x16x32_bf16 v[108:111], v[248:251], v[176:179], v[108:111]\n"
"v_mfma_f32_16x16x32_bf16 v[112:115], v[252:255], v[176:179], v[112:115]\n"
"s_waitcnt vmcnt(14)\n"
"v_mfma_f32_16x16x32_bf16 v[116:119], v[236:239], v[180:183], v[116:119]\n"
"v_mfma_f32_16x16x32_bf16 v[120:123], v[240:243], v[180:183], v[120:123]\n"
"s_add_u32 s68, s68, 4096\n"
"buffer_load_dwordx4 v[156:159], v1, s[40:43], s68 offen offset:0 sc1\n"
"v_mfma_f32_16x16x32_bf16 v[124:127], v[244:247], v[180:183], v[124:127]\n"
"v_mfma_f32_16x16x32_bf16 v[128:131], v[248:251], v[180:183], v[128:131]\n"
"v_mfma_f32_16x16x32_bf16 v[132:135], v[252:255], v[180:183], v[132:135]\n"
"s_waitcnt vmcnt(14)\n"
"v_mfma_f32_16x16x32_bf16 v[56:59], a[0:3], v[184:187], v[56:59]\n"
"v_mfma_f32_16x16x32_bf16 v[60:63], a[4:7], v[184:187], v[60:63]\n"
"s_add_u32 s69, s69, 4096\n"
"buffer_load_dwordx4 v[160:163], v1, s[40:43], s69 offen offset:0 sc1\n"
"v_mfma_f32_16x16x32_bf16 v[64:67], a[8:11], v[184:187], v[64:67]\n"
"v_mfma_f32_16x16x32_bf16 v[68:71], a[12:15], v[184:187], v[68:71]\n"
"v_mfma_f32_16x16x32_bf16 v[72:75], a[16:19], v[184:187], v[72:75]\n"
"s_waitcnt vmcnt(14)\n"
"v_mfma_f32_16x16x32_bf16 v[76:79], a[0:3], v[188:191], v[76:79]\n"
"v_mfma_f32_16x16x32_bf16 v[80:83], a[4:7], v[188:191], v[80:83]\n"
"s_add_u32 s72, s72, 4096\n"
"buffer_load_dwordx4 v[216:219], v1, s[40:43], s72 offen offset:0 sc1\n"
"v_mfma_f32_16x16x32_bf16 v[84:87], a[8:11], v[188:191], v[84:87]\n"
"v_mfma_f32_16x16x32_bf16 v[88:91], a[12:15], v[188:191], v[88:91]\n"
"v_mfma_f32_16x16x32_bf16 v[92:95], a[16:19], v[188:191], v[92:95]\n"
"s_waitcnt vmcnt(14)\n"
"v_mfma_f32_16x16x32_bf16 v[96:99], a[0:3], v[192:195], v[96:99]\n"
"v_mfma_f32_16x16x32_bf16 v[100:103], a[4:7], v[192:195], v[100:103]\n"
"s_add_u32 s68, s68, -4096\n"
"buffer_load_dwordx4 v[220:223], v20, s[40:43], s68 offen offset:2048 sc1\n"
"v_mfma_f32_16x16x32_bf16 v[104:107], a[8:11], v[192:195], v[104:107]\n"
"v_mfma_f32_16x16x32_bf16 v[108:111], a[12:15], v[192:195], v[108:111]\n"
"v_mfma_f32_16x16x32_bf16 v[112:115], a[16:19], v[192:195], v[112:115]\n"
"s_waitcnt vmcnt(14)\n"
"v_mfma_f32_16x16x32_bf16 v[116:119], a[0:3], v[136:139], v[116:119]\n"
"v_mfma_f32_16x16x32_bf16 v[120:123], a[4:7], v[136:139], v[120:123]\n"
"s_add_u32 s69, s69, -4096\n"
"buffer_load_dwordx4 v[224:227], v20, s[40:43], s69 offen offset:2048 sc1\n"
"v_mfma_f32_16x16x32_bf16 v[124:127], a[8:11], v[136:139], v[124:127]\n"
"v_mfma_f32_16x16x32_bf16 v[128:131], a[12:15], v[136:139], v[128:131]\n"
"v_mfma_f32_16x16x32_bf16 v[132:135], a[16:19], v[136:139], v[132:135]\n"
"s_waitcnt vmcnt(14)\n"
"v_mfma_f32_16x16x32_bf16 v[56:59], a[20:23], v[140:143], v[56:59]\n"
"v_mfma_f32_16x16x32_bf16 v[60:63], a[24:27], v[140:143], v[60:63]\n"
"s_add_u32 s72, s72, -4096\n"
"buffer_load_dwordx4 v[228:231], v20, s[40:43], s72 offen offset:2048 sc1\n"
"v_mfma_f32_16x16x32_bf16 v[64:67], a[28:31], v[140:143], v[64:67]\n"
"v_mfma_f32_16x16x32_bf16 v[68:71], a[32:35], v[140:143], v[68:71]\n"
"v_mfma_f32_16x16x32_bf16 v[72:75], a[36:39], v[140:143], v[72:75]\n"
"s_waitcnt vmcnt(14)\n"
"v_mfma_f32_16x16x32_bf16 v[76:79], a[20:23], v[144:147], v[76:79]\n"
"v_mfma_f32_16x16x32_bf16 v[80:83], a[24:27], v[144:147], v[80:83]\n"
"s_add_u32 s68, s68, -8192\n"
"buffer_load_dwordx4 v[232:235], v21, s[40:43], s68 offen offset:3072 sc1\n"
"v_mfma_f32_16x16x32_bf16 v[84:87], a[28:31], v[144:147], v[84:87]\n"
"v_mfma_f32_16x16x32_bf16 v[88:91], a[32:35], v[144:147], v[88:91]\n"
"v_mfma_f32_16x16x32_bf16 v[92:95], a[36:39], v[144:147], v[92:95]\n"
"s_waitcnt vmcnt(14)\n"
"v_mfma_f32_16x16x32_bf16 v[96:99], a[20:23], v[196:199], v[96:99]\n"
"v_mfma_f32_16x16x32_bf16 v[100:103], a[24:27], v[196:199], v[100:103]\n"
"s_add_u32 s69, s69, -8192\n"
"buffer_load_dwordx4 v[164:167], v21, s[40:43], s69 offen offset:3072 sc1\n"
"v_mfma_f32_16x16x32_bf16 v[104:107], a[28:31], v[196:199], v[104:107]\n"
"v_mfma_f32_16x16x32_bf16 v[108:111], a[32:35], v[196:199], v[108:111]\n"
"v_mfma_f32_16x16x32_bf16 v[112:115], a[36:39], v[196:199], v[112:115]\n"
"s_mov_b64 exec, 0xff\n"
"s_mov_b32 s67, 262144\n"
"s_waitcnt vmcnt(11)\n"
"v_cmp_ne_u64 vcc, s[56:57], v[44:45]\n"
"s_nop 3\n"
"s_cbranch_vccz PD29_%=\n"
"PL28_%=:\n"
"s_sleep 1\n"
"buffer_load_dwordx2 v[44:45], v22, s[40:43], s77 offen sc1\n"
"s_waitcnt vmcnt(0)\n"
"v_cmp_ne_u64 vcc, s[56:57], v[44:45]\n"
"s_nop 3\n"
"s_cbranch_vccz PD29_%=\n"
"PAD7_%=:\n"
"s_cbranch_execz PADRET2_%=\n"
"s_sub_u32 s67, s67, 1\n"
"s_cbranch_scc0 PL28_%=\n"
"PD29_%=:\n"
"s_mov_b64 exec, -1\n"
"v_mfma_f32_16x16x32_bf16 v[56:59], a[40:43], v[200:203], v[56:59]\n"
"v_mfma_f32_16x16x32_bf16 v[60:63], a[44:47], v[200:203], v[60:63]\n"
"s_add_u32 s72, s72, -8192\n"
"buffer_load_dwordx4 v[168:171], v21, s[40:43], s72 offen offset:3072 sc1\n"
"v_mfma_f32_16x16x32_bf16 v[64:67], a[48:51], v[200:203], v[64:67]\n"
"v_mfma_f32_16x16x32_bf16 v[68:71], a[52:55], v[200:203], v[68:71]\n"
"v_mfma_f32_16x16x32_bf16 v[72:75], a[56:59], v[200:203], v[72:75]\n"
"v_mfma_f32_16x16x32_bf16 v[76:79], a[40:43], v[204:207], v[76:79]\n"
"v_mfma_f32_16x16x32_bf16 v[80:83], a[44:47], v[204:207], v[80:83]\n"
"buffer_load_dwordx4 v[172:175], v21, s[40:43], s68 offen offset:2048 sc1\n"
"v_mfma_f32_16x16x32_bf16 v[84:87], a[48:51], v[204:207], v[84:87]\n"
"v_mfma_f32_16x16x32_bf16 v[88:91], a[52:55], v[204:207], v[88:91]\n"
"v_mfma_f32_16x16x32_bf16 v[92:95], a[56:59], v[204:207], v[92:95]\n"
"v_mfma_f32_16x16x32_bf16 v[96:99], a[40:43], v[208:211], v[96:99]\n"
"v_mfma_f32_16x16x32_bf16 v[100:103], a[44:47], v[208:211], v[100:103]\n"
"buffer_load_dwordx4 v[176:179], v21, s[40:43], s69 offen offset:2048 sc1\n"
"v_mfma_f32_16x16x32_bf16 v[104:107], a[48:51], v[208:211], v[104:107]\n"
"v_mfma_f32_16x16x32_bf16 v[108:111], a[52:55], v[208:211], v[108:111]\n"
"v_mfma_f32_16x16x32_bf16 v[112:115], a[56:59], v[208:211], v[112:115]\n"
"s_waitcnt vmcnt(13)\n"
"v_mfma_f32_16x16x32_bf16 v[56:59], a[60:63], v[212:215], v[56:59]\n"
"v_mfma_f32_16x16x32_bf16 v[60:63], a[64:67], v[212:215], v[60:63]\n"
"buffer_load_dwordx4 v[236:239], v21, s[40:43], s72 offen offset:2048 sc1\n"
"v_mfma_f32_16x16x32_bf16 v[64:67], a[68:71], v[212:215], v[64:67]\n"
"v_mfma_f32_16x16x32_bf16 v[68:71], a[72:75], v[212:215], v[68:71]\n"
"v_mfma_f32_16x16x32_bf16 v[72:75], a[76:79], v[212:215], v[72:75]\n"
"s_waitcnt vmcnt(13)\n"
"v_mfma_f32_16x16x32_bf16 v[76:79], a[60:63], v[148:151], v[76:79]\n"
"v_mfma_f32_16x16x32_bf16 v[80:83], a[64:67], v[148:151], v[80:83]\n"
"s_add_u32 s68, s68, 4096\n"
"buffer_load_dwordx4 v[240:243], v21, s[40:43], s68 offen offset:2048 sc1\n"
"v_mfma_f32_16x16x32_bf16 v[84:87], a[68:71], v[148:151], v[84:87]\n"
"v_mfma_f32_16x16x32_bf16 v[88:91], a[72:75], v[148:151], v[88:91]\n"
"v_mfma_f32_16x16x32_bf16 v[92:95], a[76:79], v[148:151], v[92:95]\n"
"s_waitcnt vmcnt(13)\n"
"v_mfma_f32_16x16x32_bf16 v[96:99], a[60:63], v[152:155], v[96:99]\n"
"v_mfma_f32_16x16x32_bf16 v[100:103], a[64:67], v[152:155], v[100:103]\n"
"s_add_u32 s68, s68, 4096\n"
"buffer_load_dwordx4 v[244:247], v21, s[40:43], s68 offen offset:1024 sc1\n"
"v_mfma_f32_16x16x32_bf16 v[104:107], a[68:71], v[152:155], v[104:107]\n"
"v_mfma_f32_16x16x32_bf16 v[108:111], a[72:75], v[152:155], v[108:111]\n"
"v_mfma_f32_16x16x32_bf16 v[112:115], a[76:79], v[152:155], v[112:115]\n"
"s_waitcnt vmcnt(13)\n"
"v_mfma_f32_16x16x32_bf16 v[56:59], a[80:83], v[156:159], v[56:59]\n"
"v_mfma_f32_16x16x32_bf16 v[60:63], a[84:87], v[156:159], v[60:63]\n"
"s_add_u32 s68, s68, 4096\n"
"buffer_load_dwordx4 v[248:251], v1, s[40:43], s68 offen offset:1024 sc1\n"
"v_mfma_f32_16x16x32_bf16 v[64:67], a[88:91], v[156:159], v[64:67]\n"
"v_mfma_f32_16x16x32_bf16 v[68:71], a[92:95], v[156:159], v[68:71]\n"
"v_mfma_f32_16x16x32_bf16 v[72:75], a[96:99], v[156:159], v[72:75]\n"
"s_waitcnt vmcnt(13)\n"
"v_mfma_f32_16x16x32_bf16 v[76:79], a[80:83], v[160:163], v[76:79]\n"
"v_mfma_f32_16x16x32_bf16 v[80:83], a[84:87], v[160:163], v[80:83]\n"
"s_add_u32 s69, s69, 4096\n"
"buffer_load_dwordx4 v[252:255], v21, s[40:43], s69 offen offset:2048 sc1\n"
"v_mfma_f32_16x16x32_bf16 v[84:87], a[88:91], v[160:163], v[84:87]\n"
"v_mfma_f32_16x16x32_bf16 v[88:91], a[92:95], v[160:163], v[88:91]\n"
"v_mfma_f32_16x16x32_bf16 v[92:95], a[96:99], v[160:163], v[92:95]\n"
"s_waitcnt vmcnt(13)\n"
"v_mfma_f32_16x16x32_bf16 v[96:99], a[80:83], v[216:219], v[96:99]\n"
"v_mfma_f32_16x16x32_bf16 v[100:103], a[84:87], v[216:219], v[100:103]\n"
"s_add_u32 s69, s69, 4096\n"
"buffer_load_dwordx4 v[180:183], v21, s[40:43], s69 offen offset:1024 sc1\n"
"v_mfma_f32_16x16x32_bf16 v[104:107], a[88:91], v[216:219], v[104:107]\n"
"v_mfma_f32_16x16x32_bf16 v[108:111], a[92:95], v[216:219], v[108:111]\n"
"v_mfma_f32_16x16x32_bf16 v[112:115], a[96:99], v[216:219], v[112:115]\n"
"s_waitcnt vmcnt(13)\n"
"v_mfma_f32_16x16x32_bf16 v[56:59], a[100:103], v[220:223], v[56:59]\n"
"v_mfma_f32_16x16x32_bf16 v[60:63], a[104:107], v[220:223], v[60:63]\n"
"s_add_u32 s69, s69, 4096\n"
"buffer_load_dwordx4 v[184:187], v1, s[40:43], s69 offen offset:1024 sc1\n"
"v_mfma_f32_16x16x32_bf16 v[64:67], a[108:111], v[220:223], v[64:67]\n"
"v_mfma_f32_16x16x32_bf16 v[68:71], a[112:115], v[220:223], v[68:71]\n"
"v_mfma_f32_16x16x32_bf16 v[72:75], a[116:119], v[220:223], v[72:75]\n"
"s_waitcnt vmcnt(13)\n"
"v_mfma_f32_16x16x32_bf16 v[76:79], a[100:103], v[224:227], v[76:79]\n"
"v_mfma_f32_16x16x32_bf16 v[80:83], a[104:107], v[224:227], v[80:83]\n"
"s_add_u32 s72, s72, 4096\n"
"buffer_load_dwordx4 v[188:191], v21, s[40:43], s72 offen offset:2048 sc1\n"
"v_mfma_f32_16x16x32_bf16 v[84:87], a[108:111], v[224:227], v[84:87]\n"
"v_mfma_f32_16x16x32_bf16 v[88:91], a[112:115], v[224:227], v[88:91]\n"
"v_mfma_f32_16x16x32_bf16 v[92:95], a[116:119], v[224:227], v[92:95]\n"
"s_waitcnt vmcnt(13)\n"
"v_mfma_f32_16x16x32_bf16 v[96:99], a[100:103], v[228:231], v[96:99]\n"
"v_mfma_f32_16x16x32_bf16 v[100:103], a[104:107], v[228:231], v[100:103]\n"
"s_add_u32 s72, s72, 4096\n"
"buffer_load_dwordx4 v[192:195], v21, s[40:43], s72 offen offset:1024 sc1\n"
"v_mfma_f32_16x16x32_bf16 v[104:107], a[108:111], v[228:231], v[104:107]\n"
"v_mfma_f32_16x16x32_bf16 v[108:111], a[112:115], v[228:231], v[108:111]\n"
"v_mfma_f32_16x16x32_bf16 v[112:115], a[116:119], v[228:231], v[112:115]\n"
"s_waitcnt vmcnt(13)\n"
"v_mfma_f32_16x16x32_bf16 v[56:59], a[120:123], v[232:235], v[56:59]\n"
"v_mfma_f32_16x16x32_bf16 v[60:63], a[124:127], v[232:235], v[60:63]\n"
"s_add_u32 s72, s72, 4096\n"
"buffer_load_dwordx4 v[136:139], v1, s[40:43], s72 offen offset:1024 sc1\n"
"v_mfma_f32_16x16x32_bf16 v[64:67], a[128:131], v[232:235], v[64:67]\n"
"v_mfma_f32_16x16x32_bf16 v[68:71], a[132:135], v[232:235], v[68:71]\n"
"v_mfma_f32_16x16x32_bf16 v[72:75], a[136:139], v[232:235], v[72:75]\n"
"s_waitcnt vmcnt(13)\n"
"v_mfma_f32_16x16x32_bf16 v[76:79], a[120:123], v[164:167], v[76:79]\n"
"v_mfma_f32_16x16x32_bf16 v[80:83], a[124:127], v[164:167], v[80:83]\n"
"buffer_load_dwordx4 v[140:143], v21, s[40:43], s73 offen offset:3072 sc1\n"
"v_mfma_f32_16x16x32_bf16 v[84:87], a[128:131], v[164:167], v[84:87]\n"
"v_mfma_f32_16x16x32_bf16 v[88:91], a[132:135], v[164:167], v[88:91]\n"
"v_mfma_f32_16x16x32_bf16 v[92:95], a[136:139], v[164:167], v[92:95]\n"
"s_waitcnt vmcnt(13)\n"
"v_mfma_f32_16x16x32_bf16 v[96:99], a[120:123], v[168:171], v[96:99]\n"
"v_mfma_f32_16x16x32_bf16 v[100:103], a[124:127], v[168:171], v[100:103]\n"
"s_add_u32 s73, s73, 4096\n"
"buffer_load_dwordx4 v[144:147], v21, s[40:43], s73 offen offset:0 sc1\n"
"v_mfma_f32_16x16x32_bf16 v[104:107], a[128:131], v[168:171], v[104:107]\n"
"v_mfma_f32_16x16x32_bf16 v[108:111], a[132:135], v[168:171], v[108:111]\n"
"v_mfma_f32_16x16x32_bf16 v[112:115], a[136:139], v[168:171], v[112:115]\n"
"s_waitcnt vmcnt(13)\n"
"v_mfma_f32_16x16x32_bf16 v[56:59], a[140:143], v[172:175], v[56:59]\n"
"v_mfma_f32_16x16x32_bf16 v[60:63], a[144:147], v[172:175], v[60:63]\n"
"buffer_load_dwordx4 v[196:199], v1, s[40:43], s73 offen offset:3072 sc1\n"
"v_mfma_f32_16x16x32_bf16 v[64:67], a[148:151], v[172:175], v[64:67]\n"
"v_mfma_f32_16x16x32_bf16 v[68:71], a[152:155], v[172:175], v[68:71]\n"
"v_mfma_f32_16x16x32_bf16 v[72:75], a[156:159], v[172:175], v[72:75]\n"
"s_waitcnt vmcnt(13)\n"
"v_mfma_f32_16x16x32_bf16 v[76:79], a[140:143], v[176:179], v[76:79]\n"
"v_mfma_f32_16x16x32_bf16 v[80:83], a[144:147], v[176:179], v[80:83]\n"
"s_add_u32 s73, s73, 4096\n"
"buffer_load_dwordx4 v[200:203], v1, s[40:43], s73 offen offset:0 sc1\n"
"v_mfma_f32_16x16x32_bf16 v[84:87], a[148:151], v[176:179], v[84:87]\n"
"v_mfma_f32_16x16x32_bf16 v[88:91], a[152:155], v[176:179], v[88:91]\n"
"v_mfma_f32_16x16x32_bf16 v[92:95], a[156:159], v[176:179], v[92:95]\n"
"s_waitcnt vmcnt(13)\n"
"v_mfma_f32_16x16x32_bf16 v[96:99], a[140:143], v[236:239], v[96:99]\n"
"v_mfma_f32_16x16x32_bf16 v[100:103], a[144:147], v[236:239], v[100:103]\n"
"s_add_u32 s73, s73, -4096\n"
"buffer_load_dwordx4 v[204:207], v20, s[40:43], s73 offen offset:2048 sc1\n"
"v_mfma_f32_16x16x32_bf16 v[104:107], a[148:151], v[236:239], v[104:107]\n"
"v_mfma_f32_16x16x32_bf16 v[108:111], a[152:155], v[236:239], v[108:111]\n"
"v_mfma_f32_16x16x32_bf16 v[112:115], a[156:159], v[236:239], v[112:115]\n"
"s_waitcnt vmcnt(13)\n"
"v_mfma_f32_16x16x32_bf16 v[56:59], a[160:163], v[240:243], v[56:59]\n"
"v_mfma_f32_16x16x32_bf16 v[60:63], a[164:167], v[240:243], v[60:63]\n"
"s_add_u32 s73, s73, -8192\n"
"buffer_load_dwordx4 v[208:211], v21, s[40:43], s73 offen offset:3072 sc1\n"
"v_mfma_f32_16x16x32_bf16 v[64:67], a[168:171], v[240:243], v[64:67]\n"
"v_mfma_f32_16x16x32_bf16 v[68:71], a[172:175], v[240:243], v[68:71]\n"
"v_mfma_f32_16x16x32_bf16 v[72:75], a[176:179], v[240:243], v[72:75]\n"
"s_waitcnt vmcnt(13)\n"
"v_mfma_f32_16x16x32_bf16 v[56:59], a[180:183], v[244:247], v[56:59]\n"
"v_mfma_f32_16x16x32_bf16 v[60:63], a[184:187], v[244:247], v[60:63]\n"
"buffer_load_dwordx4 v[212:215], v21, s[40:43], s73 offen offset:2048 sc1\n"
"v_mfma_f32_16x16x32_bf16 v[64:67], a[188:191], v[244:247], v[64:67]\n"
"v_mfma_f32_16x16x32_bf16 v[68:71], a[192:195], v[244:247], v[68:71]\n"
"v_mfma_f32_16x16x32_bf16 v[72:75], a[196:199], v[244:247], v[72:75]\n"
"s_waitcnt vmcnt(13)\n"
"v_mfma_f32_16x16x32_bf16 v[56:59], a[200:203], v[248:251], v[56:59]\n"
"v_mfma_f32_16x16x32_bf16 v[60:63], a[204:207], v[248:251], v[60:63]\n"
"s_add_u32 s73, s73, 4096\n"
"buffer_load_dwordx4 v[148:151], v21, s[40:43], s73 offen offset:2048 sc1\n"
"v_mfma_f32_16x16x32_bf16 v[64:67], a[208:211], v[248:251], v[64:67]\n"
"v_mfma_f32_16x16x32_bf16 v[68:71], a[212:215], v[248:251], v[68:71]\n"
"v_mfma_f32_16x16x32_bf16 v[72:75], a[216:219], v[248:251], v[72:75]\n"
"s_waitcnt vmcnt(13)\n"
"v_mfma_f32_16x16x32_bf16 v[76:79], a[160:163], v[252:255], v[76:79]\n"
"v_mfma_f32_16x16x32_bf16 v[80:83], a[164:167], v[252:255], v[80:83]\n"
"s_add_u32 s73, s73, 4096\n"
"buffer_load_dwordx4 v[152:155], v21, s[40:43], s73 offen offset:1024 sc1\n"
"v_mfma_f32_16x16x32_bf16 v[84:87], a[168:171], v[252:255], v[84:87]\n"
"ds_write_b128 v12, v[56:59] offset:0\n"
"v_mfma_f32_16x16x32_bf16 v[88:91], a[172:175], v[252:255], v[88:91]\n"
"v_mfma_f32_16x16x32_bf16 v[92:95], a[176:179], v[252:255], v[92:95]\n"
"ds_write_b128 v12, v[60:63] offset:1024\n"
"s_waitcnt vmcnt(13)\n"
"v_mfma_f32_16x16x32_bf16 v[76:79], a[180:183], v[180:183], v[76:79]\n"
"ds_write_b128 v12, v[64:67] offset:2048\n"
"v_mfma_f32_16x16x32_bf16 v[80:83], a[184:187], v[180:183], v[80:83]\n"
"s_add_u32 s73, s73, 4096\n"
"buffer_load_dwordx4 v[156:159], v1, s[40:43], s73 offen offset:1024 sc1\n"
"v_mfma_f32_16x16x32_bf16 v[84:87], a[188:191], v[180:183], v[84:87]\n"
"ds_write_b128 v12, v[68:71] offset:3072\n"
"v_mfma_f32_16x16x32_bf16 v[88:91], a[192:195], v[180:183], v[88:91]\n"
"v_mfma_f32_16x16x32_bf16 v[92:95], a[196:199], v[180:183], v[92:95]\n"
"ds_write_b128 v12, v[72:75] offset:4096\n"
"s_waitcnt vmcnt(13)\n"
"v_mfma_f32_16x16x32_bf16 v[76:79], a[200:203], v[184:187], v[76:79]\n"
"v_mfma_f32_16x16x32_bf16 v[80:83], a[204:207], v[184:187], v[80:83]\n"
"v_mfma_f32_16x16x32_bf16 v[84:87], a[208:211], v[184:187], v[84:87]\n"
"v_mfma_f32_16x16x32_bf16 v[88:91], a[212:215], v[184:187], v[88:91]\n"
"v_mfma_f32_16x16x32_bf16 v[92:95], a[216:219], v[184:187], v[92:95]\n"
"s_waitcnt vmcnt(12)\n"
"v_mfma_f32_16x16x32_bf16 v[96:99], a[160:163], v[188:191], v[96:99]\n"
"v_mfma_f32_16x16x32_bf16 v[100:103], a[164:167], v[188:191], v[100:103]\n"
"v_mfma_f32_16x16x32_bf16 v[104:107], a[168:171], v[188:191], v[104:107]\n"
"ds_write_b128 v13, v[76:79] offset:0\n"
"v_mfma_f32_16x16x32_bf16 v[108:111], a[172:175], v[188:191], v[108:111]\n"
"v_mfma_f32_16x16x32_bf16 v[112:115], a[176:179], v[188:191], v[112:115]\n"
"ds_write_b128 v13, v[80:83] offset:1024\n"
"s_waitcnt vmcnt(11)\n"
"v_mfma_f32_16x16x32_bf16 v[96:99], a[180:183], v[192:195], v[96:99]\n"
"ds_write_b128 v13, v[84:87] offset:2048\n"
"v_mfma_f32_16x16x32_bf16 v[100:103], a[184:187], v[192:195], v[100:103]\n"
"v_mfma_f32_16x16x32_bf16 v[104:107], a[188:191], v[192:195], v[104:107]\n"
"ds_write_b128 v13, v[88:91] offset:3072\n"
"v_mfma_f32_16x16x32_bf16 v[108:111], a[192:195], v[192:195], v[108:111]\n"
"v_mfma_f32_16x16x32_bf16 v[112:115], a[196:199], v[192:195], v[112:115]\n"
"ds_write_b128 v13, v[92:95] offset:4096\n"
"s_waitcnt vmcnt(10)\n"
"v_mfma_f32_16x16x32_bf16 v[96:99], a[200:203], v[136:139], v[96:99]\n"
"v_mfma_f32_16x16x32_bf16 v[100:103], a[204:207], v[136:139], v[100:103]\n"
"v_mfma_f32_16x16x32_bf16 v[104:107], a[208:211], v[136:139], v[104:107]\n"
"v_mfma_f32_16x16x32_bf16 v[108:111], a[212:215], v[136:139], v[108:111]\n"
"v_mfma_f32_16x16x32_bf16 v[112:115], a[216:219], v[136:139], v[112:115]\n"
"s_waitcnt vmcnt(9)\n"
"v_mfma_f32_16x16x32_bf16 v[116:119], a[20:23], v[140:143], v[116:119]\n"
"v_mfma_f32_16x16x32_bf16 v[120:123], a[24:27], v[140:143], v[120:123]\n"
"s_nop 0\n"
"ds_write_b128 v14, v[96:99] offset:0\n"
"v_mfma_f32_16x16x32_bf16 v[124:127], a[28:31], v[140:143], v[124:127]\n"
"ds_write_b128 v14, v[100:103] offset:1024\n"
"v_mfma_f32_16x16x32_bf16 v[128:131], a[32:35], v[140:143], v[128:131]\n"
"ds_write_b128 v14, v[104:107] offset:2048\n"
"v_mfma_f32_16x16x32_bf16 v[132:135], a[36:39], v[140:143], v[132:135]\n"
"ds_write_b128 v14, v[108:111] offset:3072\n"
"s_waitcnt vmcnt(8)\n"
"v_mfma_f32_16x16x32_bf16 v[116:119], a[40:43], v[144:147], v[116:119]\n"
"ds_write_b128 v14, v[112:115] offset:4096\n"
"v_mfma_f32_16x16x32_bf16 v[120:123], a[44:47], v[144:147], v[120:123]\n"
"v_mfma_f32_16x16x32_bf16 v[124:127], a[48:51], v[144:147], v[124:127]\n"
"s_waitcnt lgkmcnt(0)\n"
"s_barrier\n"
"ds_read_b128 v[160:163], v15 offset:0\n"
"v_mfma_f32_16x16x32_bf16 v[128:131], a[52:55], v[144:147], v[128:131]\n"
"ds_read_b128 v[216:219], v15 offset:5120\n"
"v_mfma_f32_16x16x32_bf16 v[132:135], a[56:59], v[144:147], v[132:135]\n"
"ds_read_b128 v[220:223], v15 offset:10240\n"
"s_waitcnt vmcnt(7)\n"
"v_mfma_f32_16x16x32_bf16 v[116:119], a[60:63], v[196:199], v[116:119]\n"
"ds_read_b128 v[224:227], v15 offset:1024\n"
"v_mfma_f32_16x16x32_bf16 v[120:123], a[64:67], v[196:199], v[120:123]\n"
"ds_read_b128 v[228:231], v15 offset:6144\n"
"v_mfma_f32_16x16x32_bf16 v[124:127], a[68:71], v[196:199], v[124:127]\n"
"ds_read_b128 v[232:235], v15 offset:11264\n"
"v_mfma_f32_16x16x32_bf16 v[128:131], a[72:75], v[196:199], v[128:131]\n"
"ds_read_b128 v[164:167], v15 offset:2048\n"
"v_mfma_f32_16x16x32_bf16 v[132:135], a[76:79], v[196:199], v[132:135]\n"
"ds_read_b128 v[168:171], v15 offset:7168\n"
"s_waitcnt vmcnt(6)\n"
"v_mfma_f32_16x16x32_bf16 v[116:119], a[80:83], v[200:203], v[116:119]\n"
"ds_read_b128 v[172:175], v15 offset:12288\n"
"v_mfma_f32_16x16x32_bf16 v[120:123], a[84:87], v[200:203], v[120:123]\n"
"ds_read_b128 v[176:179], v15 offset:3072\n"
"v_mfma_f32_16x16x32_bf16 v[124:127], a[88:91], v[200:203], v[124:127]\n"
"ds_read_b128 v[236:239], v15 offset:8192\n"
"v_mfma_f32_16x16x32_bf16 v[128:131], a[92:95], v[200:203], v[128:131]\n"
"ds_read_b128 v[240:243], v15 offset:13312\n"
"v_mfma_f32_16x16x32_bf16 v[132:135], a[96:99], v[200:203], v[132:135]\n"
"ds_read_b128 v[244:247], v15 offset:4096\n"
"s_waitcnt vmcnt(5)\n"
"v_mfma_f32_16x16x32_bf16 v[116:119], a[100:103], v[204:207], v[116:119]\n"
"ds_read_b128 v[248:251], v15 offset:9216\n"
"s_waitcnt lgkmcnt(12)\n"
"v_add_f32 v160, v160, v216\n"
"v_add_f32 v161, v161, v217\n"
"v_mfma_f32_16x16x32_bf16 v[120:123], a[104:107], v[204:207], v[120:123]\n"
"ds_read_b128 v[252:255], v15 offset:14336\n"
"v_add_f32 v162, v162, v218\n"
"v_add_f32 v163, v163, v219\n"
"v_mfma_f32_16x16x32_bf16 v[124:127], a[108:111], v[204:207], v[124:127]\n"
"s_waitcnt lgkmcnt(12)\n"
"v_add_f32 v160, v160, v220\n"
"v_add_f32 v161, v161, v221\n"
"v_mfma_f32_16x16x32_bf16 v[128:131], a[112:115], v[204:207], v[128:131]\n"
"v_and_b32 v20, 1, v9\n"
"v_mad_u32_u24 v20, v20, 24, v11\n"
"v_add_f32 v162, v162, v222\n"
"v_add_f32 v163, v163, v223\n"
"v_mfma_f32_16x16x32_bf16 v[132:135], a[116:119], v[204:207], v[132:135]\n"
"v_fmac_f32 v24, v160, v18\n"
"v_fmac_f32 v25, v161, v18\n"
"s_waitcnt vmcnt(4)\n"
"v_mfma_f32_16x16x32_bf16 v[116:119], a[120:123], v[208:211], v[116:119]\n"
"v_fmac_f32 v26, v162, v18\n"
"v_fmac_f32 v27, v163, v18\n"
"v_mfma_f32_16x16x32_bf16 v[120:123], a[124:127], v[208:211], v[120:123]\n"
"s_waitcnt lgkmcnt(10)\n"
"v_add_f32 v224, v224, v228\n"
"v_add_f32 v225, v225, v229\n"
"v_mfma_f32_16x16x32_bf16 v[124:127], a[128:131], v[208:211], v[124:127]\n"
"v_add_f32 v226, v226, v230\n"
"v_add_f32 v227, v227, v231\n"
"v_mfma_f32_16x16x32_bf16 v[128:131], a[132:135], v[208:211], v[128:131]\n"
"s_waitcnt lgkmcnt(9)\n"
"v_add_f32 v224, v224, v232\n"
"v_add_f32 v225, v225, v233\n"
"v_mfma_f32_16x16x32_bf16 v[132:135], a[136:139], v[208:211], v[132:135]\n"
"v_add_f32 v226, v226, v234\n"
"v_add_f32 v227, v227, v235\n"
"s_waitcnt vmcnt(3)\n"
"v_mfma_f32_16x16x32_bf16 v[116:119], a[140:143], v[212:215], v[116:119]\n"
"v_fmac_f32 v28, v224, v18\n"
"v_fmac_f32 v29, v225, v18\n"
"v_mfma_f32_16x16x32_bf16 v[120:123], a[144:147], v[212:215], v[120:123]\n"
"v_fmac_f32 v30, v226, v18\n"
"v_fmac_f32 v31, v227, v18\n"
"v_mfma_f32_16x16x32_bf16 v[124:127], a[148:151], v[212:215], v[124:127]\n"
"s_waitcnt lgkmcnt(7)\n"
"v_add_f32 v164, v164, v168\n"
"v_add_f32 v165, v165, v169\n"
"v_mfma_f32_16x16x32_bf16 v[128:131], a[152:155], v[212:215], v[128:131]\n"
"v_add_f32 v166, v166, v170\n"
"v_add_f32 v167, v167, v171\n"
"v_mfma_f32_16x16x32_bf16 v[132:135], a[156:159], v[212:215], v[132:135]\n"
"s_waitcnt lgkmcnt(6)\n"
"v_add_f32 v164, v164, v172\n"
"v_add_f32 v165, v165, v173\n"
"s_waitcnt vmcnt(2)\n"
"v_mfma_f32_16x16x32_bf16 v[116:119], a[160:163], v[148:151], v[116:119]\n"
"v_add_f32 v166, v166, v174\n"
"v_add_f32 v167, v167, v175\n"
"v_mfma_f32_16x16x32_bf16 v[120:123], a[164:167], v[148:151], v[120:123]\n"
"v_fmac_f32 v32, v164, v18\n"
"v_fmac_f32 v33, v165, v18\n"
"v_mfma_f32_16x16x32_bf16 v[124:127], a[168:171], v[148:151], v[124:127]\n"
"v_fmac_f32 v34, v166, v18\n"
"v_fmac_f32 v35, v167, v18\n"
"v_mfma_f32_16x16x32_bf16 v[128:131], a[172:175], v[148:151], v[128:131]\n"
"s_waitcnt lgkmcnt(4)\n"
"v_add_f32 v176, v176, v236\n"
"v_add_f32 v177, v177, v237\n"
"v_mfma_f32_16x16x32_bf16 v[132:135], a[176:179], v[148:151], v[132:135]\n"
"v_add_f32 v178, v178, v238\n"
"v_add_f32 v179, v179, v239\n"
"s_waitcnt vmcnt(1)\n"
"v_mfma_f32_16x16x32_bf16 v[116:119], a[180:183], v[152:155], v[116:119]\n"
"s_waitcnt lgkmcnt(3)\n"
"v_add_f32 v176, v176, v240\n"
"v_add_f32 v177, v177, v241\n"
"v_mfma_f32_16x16x32_bf16 v[120:123], a[184:187], v[152:155], v[120:123]\n"
"v_add_f32 v178, v178, v242\n"
"v_add_f32 v179, v179, v243\n"
"v_mfma_f32_16x16x32_bf16 v[124:127], a[188:191], v[152:155], v[124:127]\n"
"v_fmac_f32 v36, v176, v18\n"
"v_fmac_f32 v37, v177, v18\n"
"v_mfma_f32_16x16x32_bf16 v[128:131], a[192:195], v[152:155], v[128:131]\n"
"v_fmac_f32 v38, v178, v18\n"
"v_fmac_f32 v39, v179, v18\n"
"v_mfma_f32_16x16x32_bf16 v[132:135], a[196:199], v[152:155], v[132:135]\n"
"s_waitcnt lgkmcnt(1)\n"
"v_add_f32 v244, v244, v248\n"
"v_add_f32 v245, v245, v249\n"
"s_waitcnt vmcnt(0)\n"
"v_mfma_f32_16x16x32_bf16 v[116:119], a[200:203], v[156:159], v[116:119]\n"
"v_add_f32 v246, v246, v250\n"
"v_add_f32 v247, v247, v251\n"
"v_mfma_f32_16x16x32_bf16 v[120:123], a[204:207], v[156:159], v[120:123]\n"
"s_waitcnt lgkmcnt(0)\n"
"v_add_f32 v244, v244, v252\n"
"v_add_f32 v245, v245, v253\n"
"v_mfma_f32_16x16x32_bf16 v[124:127], a[208:211], v[156:159], v[124:127]\n"
"v_add_f32 v246, v246, v254\n"
"v_add_f32 v247, v247, v255\n"
"v_mfma_f32_16x16x32_bf16 v[128:131], a[212:215], v[156:159], v[128:131]\n"
"v_fmac_f32 v40, v244, v18\n"
"v_fmac_f32 v41, v245, v18\n"
"v_mfma_f32_16x16x32_bf16 v[132:135], a[216:219], v[156:159], v[132:135]\n"
"v_fmac_f32 v42, v246, v18\n"
"v_fmac_f32 v43, v247, v18\n"
"v_pk_fma_f32 v[56:57], v[116:117], v[18:19], v[24:25] op_sel:[0,0,0] op_sel_hi:[1,0,1]\n"
"v_pk_fma_f32 v[58:59], v[118:119], v[18:19], v[26:27] op_sel:[0,0,0] op_sel_hi:[1,0,1]\n"
"v_cvt_pk_bf16_f32 v76, v56, v57\n"
"v_cvt_pk_bf16_f32 v77, v58, v59\n"
"v_pk_fma_f32 v[60:61], v[120:121], v[18:19], v[28:29] op_sel:[0,0,0] op_sel_hi:[1,0,1]\n"
"v_pk_fma_f32 v[62:63], v[122:123], v[18:19], v[30:31] op_sel:[0,0,0] op_sel_hi:[1,0,1]\n"
"v_cvt_pk_bf16_f32 v78, v60, v61\n"
"v_cvt_pk_bf16_f32 v79, v62, v63\n"
"s_nop 0\n"
"v_permlane16_swap_b32 v76, v78\n"
"v_permlane16_swap_b32 v77, v79\n"
"buffer_store_dwordx4 v[76:79], v20, s[36:39], s23 offen offset:0 sc1\n"
"v_pk_fma_f32 v[64:65], v[124:125], v[18:19], v[32:33] op_sel:[0,0,0] op_sel_hi:[1,0,1]\n"
"v_pk_fma_f32 v[66:67], v[126:127], v[18:19], v[34:35] op_sel:[0,0,0] op_sel_hi:[1,0,1]\n"
"v_cvt_pk_bf16_f32 v80, v64, v65\n"
"v_cvt_pk_bf16_f32 v81, v66, v67\n"
"v_pk_fma_f32 v[68:69], v[128:129], v[18:19], v[36:37] op_sel:[0,0,0] op_sel_hi:[1,0,1]\n"
"v_pk_fma_f32 v[70:71], v[130:131], v[18:19], v[38:39] op_sel:[0,0,0] op_sel_hi:[1,0,1]\n"
"v_cvt_pk_bf16_f32 v82, v68, v69\n"
"v_cvt_pk_bf16_f32 v83, v70, v71\n"
"s_nop 0\n"
"v_permlane16_swap_b32 v80, v82\n"
"v_permlane16_swap_b32 v81, v83\n"
"buffer_store_dwordx4 v[80:83], v20, s[36:39], s23 offen offset:64 sc1\n"
"v_pk_fma_f32 v[72:73], v[132:133], v[18:19], v[40:41] op_sel:[0,0,0] op_sel_hi:[1,0,1]\n"
"v_pk_fma_f32 v[74:75], v[134:135], v[18:19], v[42:43] op_sel:[0,0,0] op_sel_hi:[1,0,1]\n"
"v_cvt_pk_bf16_f32 v84, v72, v73\n"
"v_cvt_pk_bf16_f32 v85, v74, v75\n"
"buffer_store_dwordx2 v[84:85], v11, s[36:39], s23 offen offset:128 sc1\n"
"s_nop 1\n"
"END_%=:\n"
"s_endpgm\n"
  :: "s"(ka), "s"(b), "v"(tid), "s"(l0)
  : "memory", "vcc", "scc", "v1", "v2", "v3", "v4", "v5", "v6", "v7", "v8", "v9", "v10", "v11", "v12", "v13", "v14", "v15", "v16", "v17", "v18", "v19", "v20", "v21", "v22", "v23", "v24", "v25", "v26", "v27", "v28", "v29", "v30", "v31", "v32", "v33", "v34", "v35", "v36", "v37", "v38", "v39", "v40", "v41", "v42", "v43", "v44", "v45", "v46", "v47", "v48", "v49", "v50", "v51", "v52", "v53", "v54", "v55", "v56", "v57", "v58", "v59", "v60", "v61", "v62", "v63", "v64", "v65", "v66", "v67", "v68", "v69", "v70", "v71", "v72", "v73", "v74", "v75", "v76", "v77", "v78", "v79", "v80", "v81", "v82", "v83", "v84", "v85", "v86", "v87", "v88", "v89", "v90", "v91", "v92", "v93", "v94", "v95", "v96", "v97", "v98", "v99", "v100", "v101", "v102", "v103", "v104", "v105", "v106", "v107", "v108", "v109", "v110", "v111", "v112", "v113", "v114", "v115", "v116", "v117", "v118", "v119", "v120", "v121", "v122", "v123", "v124", "v125", "v126", "v127", "v128", "v129", "v130", "v131", "v132", "v133", "v134", "v135", "v136", "v137", "v138", "v139", "v140", "v141", "v142", "v143", "v144", "v145", "v146", "v147", "v148", "v149", "v150", "v151", "v152", "v153", "v154", "v155", "v156", "v157", "v158", "v159", "v160", "v161", "v162", "v163", "v164", "v165", "v166", "v167", "v168", "v169", "v170", "v171", "v172", "v173", "v174", "v175", "v176", "v177", "v178", "v179", "v180", "v181", "v182", "v183", "v184", "v185", "v186", "v187", "v188", "v189", "v190", "v191", "v192", "v193", "v194", "v195", "v196", "v197", "v198", "v199", "v200", "v201", "v202", "v203", "v204", "v205", "v206", "v207", "v208", "v209", "v210", "v211", "v212", "v213", "v214", "v215", "v216", "v217", "v218", "v219", "v220", "v221", "v222", "v223", "v224", "v225", "v226", "v227", "v228", "v229", "v230", "v231", "v232", "v233", "v234", "v235", "v236", "v237", "v238", "v239", "v240", "v241", "v242", "v243", "v244", "v245", "v246", "v247", "v248", "v249", "v250", "v251", "v252", "v253", "v254", "v255", "a0", "a1", "a2", "a3", "a4", "a5", "a6", "a7", "a8", "a9", "a10", "a11", "a12", "a13", "a14", "a15", "a16", "a17", "a18", "a19", "a20", "a21", "a22", "a23", "a24", "a25", "a26", "a27", "a28", "a29", "a30", "a31", "a32", "a33", "a34", "a35", "a36", "a37", "a38", "a39", "a40", "a41", "a42", "a43", "a44", "a45", "a46", "a47", "a48", "a49", "a50", "a51", "a52", "a53", "a54", "a55", "a56", "a57", "a58", "a59", "a60", "a61", "a62", "a63", "a64", "a65", "a66", "a67", "a68", "a69", "a70", "a71", "a72", "a73", "a74", "a75", "a76", "a77", "a78", "a79", "a80", "a81", "a82", "a83", "a84", "a85", "a86", "a87", "a88", "a89", "a90", "a91", "a92", "a93", "a94", "a95", "a96", "a97", "a98", "a99", "a100", "a101", "a102", "a103", "a104", "a105", "a106", "a107", "a108", "a109", "a110", "a111", "a112", "a113", "a114", "a115", "a116", "a117", "a118", "a119", "a120", "a121", "a122", "a123", "a124", "a125", "a126", "a127", "a128", "a129", "a130", "a131", "a132", "a133", "a134", "a135", "a136", "a137", "a138", "a139", "a140", "a141", "a142", "a143", "a144", "a145", "a146", "a147", "a148", "a149", "a150", "a151", "a152", "a153", "a154", "a155", "a156", "a157", "a158", "a159", "a160", "a161", "a162", "a163", "a164", "a165", "a166", "a167", "a168", "a169", "a170", "a171", "a172", "a173", "a174", "a175", "a176", "a177", "a178", "a179", "a180", "a181", "a182", "a183", "a184", "a185", "a186", "a187", "a188", "a189", "a190", "a191", "a192", "a193", "a194", "a195", "a196", "a197", "a198", "a199", "a200", "a201", "a202", "a203", "a204", "a205", "a206", "a207", "a208", "a209", "a210", "a211", "a212", "a213", "a214", "a215", "a216", "a217", "a218", "a219", "a220", "a221", "a222", "a223", "a224", "a225", "a226", "a227", "a228", "a229", "a230", "a231", "a232", "a233", "a234", "a235", "a236", "a237", "a238", "a239", "a240", "a241", "a242", "a243", "a244", "a245", "a246", "a247", "a248", "a249", "a250", "a251", "a252", "a253", "a254", "a255", "s4", "s5", "s6", "s7", "s8", "s9", "s10", "s11", "s12", "s13", "s14", "s15", "s16", "s17", "s18", "s19", "s20", "s21", "s22", "s23", "s24", "s25", "s26", "s27", "s28", "s29", "s30", "s31", "s36", "s37", "s38", "s39", "s40", "s41", "s42", "s43", "s44", "s45", "s46", "s47", "s48", "s49", "s50", "s51", "s52", "s53", "s54", "s55", "s56", "s57", "s58", "s59", "s60", "s61", "s62", "s63", "s64", "s65", "s66", "s67", "s68", "s69", "s70", "s71", "s72", "s73", "s74", "s75", "s76", "s77", "s78", "s79", "s80", "s81", "s82", "s83", "s84", "s85", "s86", "s87", "s88", "s89", "s90", "s91", "s92", "s93", "s94", "s95", "s96", "s97", "s98", "s99");
}

extern "C" void kernel_launch(void* const* d_in, const int* in_sizes, int n_in, void* d_out, int out_size,
                              void* d_ws, size_t ws_size, hipStream_t stream) {
  fused_block<<<256, 256, 0, stream>>>(d_in[0], d_in[1], d_in[2], d_in[3], d_in[5], d_in[6], d_out, (char*)d_ws + 0ull);
}
```
